```python
import torch

def _softmax(x, axis=-1):
    return torch.softmax(x, dim=axis)
def _gelu(x):
    return torch.nn.functional.gelu(x, approximate="tanh")
def _silu(x):
    return torch.nn.functional.silu(x)
def _sigmoid(x):
    return torch.sigmoid(x)
def _layernorm(x, axis=-1, eps=1e-5):
    m = x.mean(dim=axis, keepdim=True)
    v = x.var(dim=axis, keepdim=True, unbiased=False)
    return (x - m) / torch.sqrt(v + eps)
def _scatter_add(tgt, idx, src, axis=0):
    return tgt.clone().index_add_(axis, idx, src)
def _segment_sum(src, seg, n):
    out = torch.zeros((n,) + src.shape[1:], dtype=src.dtype)
    return out.index_add_(0, seg, src)
def _pad(x, pw):
    flat = tuple(v for pair in reversed(pw) for v in pair)
    return torch.nn.functional.pad(x, flat)

kernel_name = 'concat_heads__mha_block_cached__mha_block_cached__near_ridge'

def setup_inputs():

    _w = (lambda a: (lambda t: t.to(torch.bfloat16) if t.is_floating_point() else t)(torch.as_tensor(a)))
    def _A_setup():
        B, Sq, D = 8, 128, 576
        H, Sk, Dh = 32, 512, 128
        return {
            "x": _w((lambda *s: torch.randn(*s, dtype=torch.bfloat16) * 0.8)(B, Sq, D)),
            "k_cache": _w((lambda *s: torch.randn(*s, dtype=torch.bfloat16) * 0.8)(B, H, Sk, Dh)),
            "v_cache": _w((lambda *s: torch.randn(*s, dtype=torch.bfloat16) * 0.05)(B, H, Sk, Dh)),
            "wq": _w((lambda *s: torch.randn(*s, dtype=torch.bfloat16) * 0.8)(D, H * Dh) * (D ** -0.5)),
            "wo": _w((lambda *s: torch.randn(*s, dtype=torch.bfloat16) * 0.05)(H * Dh, D) * ((H * Dh) ** -0.5)),
        }
    _A_in = _A_setup()

    def _B_setup():
        B, Sq, D = 8, 128, 576
        H, Sk, Dh = 32, 512, 128
        return {
            "x": _w((lambda *s: torch.randn(*s, dtype=torch.bfloat16) * 0.8)(B, Sq, D)),
            "k_cache": _w((lambda *s: torch.randn(*s, dtype=torch.bfloat16) * 0.8)(B, H, Sk, Dh)),
            "v_cache": _w((lambda *s: torch.randn(*s, dtype=torch.bfloat16) * 0.05)(B, H, Sk, Dh)),
            "wq": _w((lambda *s: torch.randn(*s, dtype=torch.bfloat16) * 0.8)(D, H * Dh) * (D ** -0.5)),
            "wo": _w((lambda *s: torch.randn(*s, dtype=torch.bfloat16) * 0.05)(H * Dh, D) * ((H * Dh) ** -0.5)),
        }
    _B_in = _B_setup()

    return {
        "A_x": _A_in["x"],
        "A_k_cache": _A_in["k_cache"],
        "A_v_cache": _A_in["v_cache"],
        "A_wq": _A_in["wq"],
        "A_wo": _A_in["wo"],
        "B_x": _B_in["x"],
        "B_k_cache": _B_in["k_cache"],
        "B_v_cache": _B_in["v_cache"],
        "B_wq": _B_in["wq"],
        "B_wo": _B_in["wo"],
    }

def reference(A_x, A_k_cache, A_v_cache, A_wq, A_wo, B_x, B_k_cache, B_v_cache, B_wq, B_wo):
    def _A_ref(x, k_cache, v_cache, wq, wo):
        B, Sq, D = x.shape
        H, Dh = 32, 128
        q = (lambda x, *d: x.permute(*d))((x @ wq).reshape(B, Sq, H, Dh), 0, 2, 1, 3)
        scores = torch.einsum("bhqd,bhkd->bhqk", q, k_cache) * (Dh ** -0.5)
        p = _softmax(scores, axis=-1)
        o = torch.einsum("bhqk,bhkd->bhqd", p, v_cache)
        return (lambda x, *d: x.permute(*d))(o, 0, 2, 1, 3).reshape(B, Sq, H * Dh) @ wo

    def _B_ref(x, k_cache, v_cache, wq, wo):
        B, Sq, D = x.shape
        H, Dh = 32, 128
        q = (lambda x, *d: x.permute(*d))((x @ wq).reshape(B, Sq, H, Dh), 0, 2, 1, 3)
        scores = torch.einsum("bhqd,bhkd->bhqk", q, k_cache) * (Dh ** -0.5)
        p = _softmax(scores, axis=-1)
        o = torch.einsum("bhqk,bhkd->bhqd", p, v_cache)
        return (lambda x, *d: x.permute(*d))(o, 0, 2, 1, 3).reshape(B, Sq, H * Dh) @ wo

    a = _A_ref(A_x, A_k_cache, A_v_cache, A_wq, A_wo)
    b = _B_ref(B_x, B_k_cache, B_v_cache, B_wq, B_wo)
    return torch.concatenate([a, b], axis=-1)
```

```cpp
#include <hip/hip_runtime.h>
#include <hip/hip_bf16.h>
typedef __hip_bfloat16 bf16;
constexpr int B=8,SQ=128,D=576,H=32,SK=512,DH=128;
__device__ __forceinline__ float b2f(unsigned short u){return __uint_as_float(((unsigned)u)<<16);}
__device__ __forceinline__ unsigned short f2b(float x){unsigned u=__float_as_uint(x);u+=0x7fffu+((u>>16)&1u);return (unsigned short)(u>>16);}
__global__ void k_qproj(const unsigned short* x,const unsigned short* wq,unsigned short* q){
  int idx=blockIdx.x*256+threadIdx.x;
  int n=idx%(H*DH); int row=idx/(H*DH);
  float acc=0;
  for(int k=0;k<D;++k) acc+=b2f(x[row*D+k])*b2f(wq[(size_t)k*H*DH+n]);
  q[idx]=f2b(acc);
}
__global__ void k_attn(const unsigned short* q,const unsigned short* K,const unsigned short* V,unsigned short* o){
  __shared__ float s[SK]; __shared__ float qs[DH]; __shared__ float red[128];
  int row=blockIdx.x; int h=blockIdx.y; int b=row/SQ; int t=threadIdx.x;
  qs[t]=b2f(q[(size_t)row*H*DH+h*DH+t]); __syncthreads();
  const unsigned short* Kh=K+((size_t)(b*H+h))*SK*DH; const unsigned short* Vh=V+((size_t)(b*H+h))*SK*DH;
  float mx=-1e30f;
  for(int j=t;j<SK;j+=128){float a=0;for(int d=0;d<DH;++d)a+=qs[d]*b2f(Kh[j*DH+d]);a*=0.08838834764831845f;s[j]=a;mx=fmaxf(mx,a);}
  red[t]=mx;__syncthreads();
  for(int w=64;w>0;w>>=1){if(t<w)red[t]=fmaxf(red[t],red[t+w]);__syncthreads();}
  mx=red[0];__syncthreads();
  float sum=0;for(int j=t;j<SK;j+=128){float e=__expf(s[j]-mx);s[j]=e;sum+=e;}
  red[t]=sum;__syncthreads();
  for(int w=64;w>0;w>>=1){if(t<w)red[t]+=red[t+w];__syncthreads();}
  sum=red[0];
  float a=0;for(int j=0;j<SK;++j)a+=s[j]*b2f(Vh[j*DH+t]);
  o[(size_t)row*H*DH+h*DH+t]=f2b(a/sum);
}
__global__ void k_oproj(const unsigned short* o,const unsigned short* wo,unsigned short* out,int coloff){
  int idx=blockIdx.x*256+threadIdx.x;
  int n=idx%D,row=idx/D; float acc=0;
  for(int k=0;k<H*DH;++k)acc+=b2f(o[(size_t)row*H*DH+k])*b2f(wo[(size_t)k*D+n]);
  out[(size_t)row*2*D+coloff+n]=f2b(acc);
}
extern "C" void kernel_launch(void* const* d_in,const int* in_sizes,int n_in,void* d_out,int out_size,void* d_ws,size_t ws_size,hipStream_t stream){
  unsigned short* ws=(unsigned short*)d_ws;
  for(int br=0;br<2;++br){
    const unsigned short* x=(const unsigned short*)d_in[br*5+0];const unsigned short* K=(const unsigned short*)d_in[br*5+1];
    const unsigned short* V=(const unsigned short*)d_in[br*5+2];const unsigned short* wq=(const unsigned short*)d_in[br*5+3];const unsigned short* wo=(const unsigned short*)d_in[br*5+4];
    unsigned short* q=ws+(size_t)br*2*B*SQ*H*DH; unsigned short* o=q+(size_t)B*SQ*H*DH;
    k_qproj<<<B*SQ*H*DH/256,256,0,stream>>>(x,wq,q);
    k_attn<<<dim3(B*SQ,H),128,0,stream>>>(q,K,V,o);
    k_oproj<<<B*SQ*D/256,256,0,stream>>>(o,wo,(unsigned short*)d_out,br*D);
  }
}
```

```cpp
#include <hip/hip_runtime.h>
#include <stdint.h>

#define LAS __attribute__((address_space(3)))
#define GAS __attribute__((address_space(1)))
#define DI __device__ __forceinline__
typedef __attribute__((ext_vector_type(8))) __bf16 bf16x8;
typedef __attribute__((ext_vector_type(4))) __bf16 bf16x4;
typedef __attribute__((ext_vector_type(2))) __bf16 bf16x2;
typedef __attribute__((ext_vector_type(16))) float f32x16;
typedef __attribute__((ext_vector_type(4))) float f32x4;
typedef __attribute__((ext_vector_type(2))) float f32x2;
typedef __attribute__((ext_vector_type(4))) unsigned u32x4;
typedef __attribute__((ext_vector_type(2))) unsigned u32x2;
typedef __amdgpu_buffer_rsrc_t rsrc_t;

constexpr int NB = 8, SQ = 128, DM = 576, NH = 32, SK = 512, DH = 128;
constexpr int SLOT = 16384;
constexpr size_t O_BYTES = (size_t)2 * NB * NH * SQ * DH * 2;
constexpr size_t WS_O = 0, WS_P = O_BYTES;
constexpr float CSC = 0.08838834764831845f * 1.4426950408889634f;
constexpr float THR = 8.0f;

DI unsigned cvtpk(float lo, float hi) { f32x2 v = {lo, hi}; bf16x2 b = __builtin_convertvector(v, bf16x2); return __builtin_bit_cast(unsigned, b); }
DI rsrc_t mkrsrc(const void* p, unsigned bytes) { return __builtin_amdgcn_make_buffer_rsrc((void*)p, (short)0, (int)bytes, 0x00020000); }
template <bool NT> DI void dma16(unsigned ldsaddr, unsigned voff, rsrc_t r, unsigned soff) {
  if constexpr (NT) asm volatile("s_mov_b32 m0, %0\n\ts_nop 0\n\tbuffer_load_dwordx4 %1, %2, %3 offen nt lds" ::"s"(ldsaddr), "v"(voff), "s"(r), "s"(soff) : "memory");
  else asm volatile("s_mov_b32 m0, %0\n\ts_nop 0\n\tbuffer_load_dwordx4 %1, %2, %3 offen lds" ::"s"(ldsaddr), "v"(voff), "s"(r), "s"(soff) : "memory");
}
#define WAITV(n) asm volatile("s_waitcnt vmcnt(" #n ")" ::: "memory")
#define BARRIER() asm volatile("s_waitcnt lgkmcnt(0)\n\ts_barrier" ::: "memory")
DI bf16x8 lds_b128(unsigned a) { return *(LAS bf16x8*)(size_t)a; }
DI bf16x4 lds_tr(unsigned a) { return __builtin_amdgcn_ds_read_tr16_b64_v4bf16((LAS bf16x4*)(size_t)a); }
DI bf16x8 cat(bf16x4 a, bf16x4 b) { return __builtin_shufflevector(a, b, 0, 1, 2, 3, 4, 5, 6, 7); }
DI float swapmax(float v) { auto r = __builtin_amdgcn_permlane32_swap(__float_as_uint(v), __float_as_uint(v), false, false); return fmaxf(__uint_as_float(r[0]), __uint_as_float(r[1])); }
DI float swapsum(float v) { auto r = __builtin_amdgcn_permlane32_swap(__float_as_uint(v), __float_as_uint(v), false, false); return __uint_as_float(r[0]) + __uint_as_float(r[1]); }
#define MFMA32(a, b, c) __builtin_amdgcn_mfma_f32_32x32x16_bf16(a, b, c, 0, 0, 0)
#define MFMA16(a, b, c) __builtin_amdgcn_mfma_f32_16x16x32_bf16(a, b, c, 0, 0, 0)

#define KV_NT true
#define W_NT false
#define KVSTART 1

__global__ __launch_bounds__(512, 2) void k1_qattn(const char* __restrict__ xA, const char* __restrict__ xB, const char* __restrict__ kA, const char* __restrict__ kB,
                                                   const char* __restrict__ vA, const char* __restrict__ vB, const char* __restrict__ wqA, const char* __restrict__ wqB,
                                                   char* __restrict__ Ows) {
  extern __shared__ __attribute__((aligned(16))) char lds[];
  const unsigned tid = threadIdx.x, lane = tid & 63u;
  const unsigned wid = __builtin_amdgcn_readfirstlane(tid >> 6);
  const unsigned bi = blockIdx.x, xcd = bi & 7u, r = bi >> 3, br = xcd >> 2, hg = xcd & 3u, b = r & 7u, hp = r >> 3, h0 = hg * 8u + hp * 2u;
  const unsigned L0 = (unsigned)(size_t)lds;
  const rsrc_t rx = mkrsrc((br ? xB : xA) + (size_t)b * SQ * DM * 2, SQ * DM * 2);
  const rsrc_t rw = mkrsrc(br ? wqB : wqA, DM * NH * DH * 2);
  const rsrc_t rk = mkrsrc((br ? kB : kA) + (size_t)(b * NH + h0) * SK * DH * 2, 2 * SK * DH * 2);
  const rsrc_t rv = mkrsrc((br ? vB : vA) + (size_t)(b * NH + h0) * SK * DH * 2, 2 * SK * DH * 2);
  const unsigned l15 = lane & 15u, l31 = lane & 31u, hi = lane >> 5, g4 = lane >> 4, blk = g4 & 1u, qq = l15 >> 2, pp = lane & 3u;
  const unsigned hsel = wid >> 2, qb = wid & 3u;

  const unsigned vx0 = (lane >> 3) * 1152u + (((lane & 7u) ^ (g4)) << 4), vx1 = (lane >> 3) * 1152u + (((lane & 7u) ^ (4u + g4)) << 4);
  const unsigned vw = g4 * 8192u + ((l15 ^ (g4 << 2)) << 4);
  const unsigned vv = g4 * 256u + ((l15 ^ (g4 << 2)) << 4);
  auto vk = [&](unsigned p) { return g4 * 256u + ((l15 ^ ((4u * (p & 3u) + g4) & 15u)) << 4); };

  auto stage = [&](int s) {
    const unsigned buf = L0 + (unsigned)(((s + 1) & 1) * 3) * SLOT;
#pragma unroll
    for (unsigned j = 0; j < 4; ++j) { const unsigned p = wid * 4u + j; dma16<W_NT>(buf + p * 1024u, (j & 1) ? vx1 : vx0, rx, p * 8u * 1152u + (unsigned)s * 128u); }
#pragma unroll
    for (unsigned h = 0; h < 2; ++h)
#pragma unroll
      for (unsigned j = 0; j < 4; ++j) { const unsigned p = wid * 4u + j; dma16<W_NT>(buf + (1u + h) * SLOT + p * 1024u, vw, rw, ((unsigned)s * 64u + 4u * p) * 8192u + (h0 + h) * 256u); }
  };
  auto kv_piece = [&](unsigned n, unsigned slot, unsigned p, unsigned voffk) {
    const unsigned t = n >> 2, hs = n & 1u, so = hs * 131072u + t * 16384u + p * 1024u;
    if (n & 2u) dma16<KV_NT>(L0 + slot * SLOT + p * 1024u, vv, rv, so);
    else dma16<KV_NT>(L0 + slot * SLOT + p * 1024u, voffk, rk, so);
  };
  const unsigned vkA = vk(2u * wid), vkB = vk(2u * wid + 1u);
  auto kv_tile = [&](unsigned n, unsigned slot) { kv_piece(n, slot, 2u * wid, vkA); kv_piece(n, slot, 2u * wid + 1u, vkB); };

  if (wid < 4) stage(0);
  f32x16 acc[4] = {};
  const unsigned xrow = 32u * qb + l31;
  unsigned ax[4], aw[4];
#pragma unroll
  for (unsigned ks = 0; ks < 4; ++ks) ax[ks] = xrow * 128u + (((2u * ks + hi) ^ ((xrow >> 1) & 7u)) << 4);
#pragma unroll
  for (unsigned db = 0; db < 4; ++db) aw[db] = 256u * (8u * hi + qq) + (((4u * db + 2u * blk + (pp & 1u)) ^ (qq << 2)) << 4) + 8u * (pp >> 1) + (1u + hsel) * SLOT;
#pragma unroll 1
  for (int s = 0; s < 9; ++s) {
    if (wid < 4) WAITV(0);
    BARRIER();
    if (wid < 4) { if (s < 8) stage(s + 1); }
    else if (s == KVSTART) {
#pragma unroll
      for (unsigned n = 0; n < 4; ++n)
#pragma unroll
        for (unsigned j = 0; j < 4; ++j) { const unsigned p = (wid - 4u) * 4u + j; kv_piece(n, 6u + n, p, vk(p)); }
    }
    if (s == 8) { kv_tile(4, 0); kv_tile(5, 1); kv_tile(6, 2); }
    const unsigned buf = L0 + (unsigned)(((s + 1) & 1) * 3) * SLOT;
#pragma unroll
    for (unsigned ks = 0; ks < 4; ++ks) {
      const bf16x8 bx = lds_b128(buf + ax[ks]);
#pragma unroll
      for (unsigned db = 0; db < 4; ++db) {
        const bf16x8 a = cat(lds_tr(buf + aw[db] + 256u * (16u * ks)), lds_tr(buf + aw[db] + 256u * (16u * ks + 4u)));
        acc[db] = MFMA32(a, bx, acc[db]);
      }
    }
  }
  bf16x8 qf[8];
#pragma unroll
  for (int s = 0; s < 8; ++s) {
    u32x4 w;
#pragma unroll
    for (int e = 0; e < 4; ++e) w[e] = cvtpk(acc[s >> 1][8 * (s & 1) + 2 * e], acc[s >> 1][8 * (s & 1) + 2 * e + 1]);
    qf[s] = __builtin_bit_cast(bf16x8, w);
  }

  unsigned ak[8], av[4];
#pragma unroll
  for (unsigned s = 0; s < 8; ++s) ak[s] = 256u * l31 + (((2u * s + hi) ^ l15) << 4);
#pragma unroll
  for (unsigned db = 0; db < 4; ++db) av[db] = 256u * (4u * hi + qq) + (((4u * db + 2u * blk + (pp >> 1)) ^ (qq << 2)) << 4) + 8u * (pp & 1u);
  f32x16 o[4] = {};
  float m = 0.f, l = 0.f;
  unsigned sK = 6u + hsel, sV = 8u + hsel;
  unsigned snext = 3u;
#pragma unroll 1
  for (int t = 0; t < 8; ++t) {
    if (t == 0) WAITV(14); else if (t == 7) WAITV(4); else WAITV(12);
    BARRIER();
    if (t == 0) { kv_tile(7, 3); snext = 4u; }
    if (t < 6) { kv_tile(4u * t + 8u, snext); snext = snext == 9u ? 0u : snext + 1u; kv_tile(4u * t + 9u, snext); snext = snext == 9u ? 0u : snext + 1u; }
    const unsigned kb0 = L0 + sK * SLOT;
    f32x16 S[2];
#pragma unroll
    for (int kb = 0; kb < 2; ++kb) {
      S[kb] = f32x16{};
#pragma unroll
      for (int s = 0; s < 8; ++s) S[kb] = MFMA32(lds_b128(kb0 + ak[s] + 8192u * kb), qf[s], S[kb]);
    }
    float pm = fmaxf(S[0][0], S[1][0]);
#pragma unroll
    for (int e = 1; e < 16; ++e) pm = fmaxf(pm, fmaxf(S[0][e], S[1][e]));
    pm = swapmax(pm) * CSC;
    if (t == 0) m = pm;
    else if (__builtin_expect(__any(pm - m > THR), 0)) {
      const float mn = fmaxf(m, pm), al = __builtin_amdgcn_exp2f(m - mn);
      m = mn; l *= al;
#pragma unroll
      for (int db = 0; db < 4; ++db)
#pragma unroll
        for (int e = 0; e < 16; ++e) o[db][e] *= al;
    }
    const float nm = -m;
    bf16x8 pf[4];
#pragma unroll
    for (int kb = 0; kb < 2; ++kb) {
#pragma unroll
      for (int e = 0; e < 16; ++e) { S[kb][e] = __builtin_amdgcn_exp2f(fmaf(S[kb][e], CSC, nm)); l += S[kb][e]; }
#pragma unroll
      for (int u = 0; u < 2; ++u) {
        u32x4 w;
#pragma unroll
        for (int e = 0; e < 4; ++e) w[e] = cvtpk(S[kb][8 * u + 2 * e], S[kb][8 * u + 2 * e + 1]);
        pf[2 * kb + u] = __builtin_bit_cast(bf16x8, w);
      }
    }
    if (t == 7) WAITV(0); else if (t == 6) WAITV(8); else WAITV(12);
    BARRIER();
    if (t < 6) { kv_tile(4u * t + 10u, snext); snext = snext == 9u ? 0u : snext + 1u; kv_tile(4u * t + 11u, snext); snext = snext == 9u ? 0u : snext + 1u; }
    const unsigned vb0 = L0 + sV * SLOT;
#pragma unroll
    for (int ks = 0; ks < 4; ++ks)
#pragma unroll
      for (int db = 0; db < 4; ++db) {
        const bf16x8 a = cat(lds_tr(vb0 + av[db] + 256u * (16u * ks)), lds_tr(vb0 + av[db] + 256u * (16u * ks + 8u)));
        o[db] = MFMA32(a, pf[ks], o[db]);
      }
    sK += 4u; if (sK >= 10u) sK -= 10u;
    sV += 4u; if (sV >= 10u) sV -= 10u;
  }
  l = swapsum(l);
  const float inv = __builtin_amdgcn_rcpf(l);
  char* ob = Ows + ((((size_t)(br * NB + b) * NH + (h0 + hsel)) * 8u + (2u * qb + (l31 >> 4))) * 4u) * 1024u + (l15 + 16u * hi) * 16u;
#pragma unroll
  for (int db = 0; db < 4; ++db)
#pragma unroll
    for (int u = 0; u < 2; ++u) {
      u32x4 w;
#pragma unroll
      for (int e = 0; e < 4; ++e) w[e] = cvtpk(o[db][8 * u + 2 * e] * inv, o[db][8 * u + 2 * e + 1] * inv);
      *(GAS u32x4*)(ob + db * 1024 + u * 512) = w;
    }
}

__global__ __launch_bounds__(512, 2) void k2_oproj(const char* __restrict__ Ows, const char* __restrict__ woA, const char* __restrict__ woB, char* __restrict__ Pws) {
  extern __shared__ __attribute__((aligned(16))) char lds[];
  const unsigned tid = threadIdx.x, lane = tid & 63u;
  const unsigned wid = __builtin_amdgcn_readfirstlane(tid >> 6);
  const unsigned bi = blockIdx.x, xcd = bi & 7u, r = bi >> 3, br = xcd >> 2, hg = xcd & 3u, b = r & 7u, j = r >> 3;
  const unsigned L0 = (unsigned)(size_t)lds;
  const unsigned qb = wid & 3u, kh = wid >> 2;
  const rsrc_t rw = mkrsrc(br ? woB : woA, NH * DH * DM * 2);
  unsigned vw[9];
#pragma unroll
  for (unsigned p = 0; p < 9; ++p) { const unsigned c = 64u * p + lane; vw[p] = (c / 18u) * 1152u + (c % 18u) * 16u; }
  auto stage = [&](int st) {
    const unsigned head = hg * 8u + kh * 4u + (unsigned)st;
    const unsigned dst = L0 + (unsigned)(st & 1) * 73728u + kh * 36864u + qb * 9216u, so = (head * 128u + 32u * qb) * 1152u + j * 288u;
#pragma unroll
    for (unsigned p = 0; p < 9; ++p) dma16<W_NT>(dst + p * 1024u, vw[p], rw, so);
  };
  const char* ob = Ows + (((size_t)(br * NB + b) * NH + hg * 8u + kh * 4u) * 8u + 2u * qb) * 4096u + lane * 16u;
  bf16x8 bf[2][8];
  auto loadB = [&](int st, bf16x8* d) {
#pragma unroll
    for (int f = 0; f < 8; ++f) d[f] = *(const GAS bf16x8*)(ob + (size_t)st * 32768u + f * 1024);
  };
  const unsigned aw = 288u * (16u * (lane >> 5) + 4u * ((lane >> 4) & 1u) + ((lane & 15u) >> 2)) + 8u * (lane & 3u) + kh * 36864u;
  f32x4 acc[9][2] = {};
  stage(0); loadB(0, bf[0]);
#pragma unroll
  for (int st = 0; st < 4; ++st) {
    WAITV(0);
    BARRIER();
    bf16x8* cur = bf[st & 1];
#pragma unroll
    for (int f = 0; f < 8; ++f) asm volatile("" : "+v"(cur[f]));
    if (st < 3) { stage(st + 1); loadB(st + 1, bf[(st + 1) & 1]); }
    const unsigned buf = L0 + (unsigned)(st & 1) * 73728u + aw;
#pragma unroll
    for (int s = 0; s < 4; ++s)
#pragma unroll
      for (int nb = 0; nb < 9; ++nb) {
        const bf16x8 a = cat(lds_tr(buf + 288u * (32u * s) + 32u * nb), lds_tr(buf + 288u * (32u * s + 8u) + 32u * nb));
        acc[nb][0] = MFMA16(a, cur[s], acc[nb][0]);
        acc[nb][1] = MFMA16(a, cur[4 + s], acc[nb][1]);
      }
  }
  BARRIER();
  LAS f32x4* cb = (LAS f32x4*)(size_t)(L0 + qb * 18432u + lane * 16u);
  if (kh == 1) {
#pragma unroll
    for (int nb = 0; nb < 9; ++nb) { cb[(0 * 9 + nb) * 64] = acc[nb][0]; cb[(1 * 9 + nb) * 64] = acc[nb][1]; }
  }
  BARRIER();
  if (kh == 0) {
    GAS f32x4* pb = (GAS f32x4*)(Pws + ((((size_t)(br * NB + b) * 4u + j) * 8u + 2u * qb) * 9u * 4u + hg) * 1024u + lane * 16u);
#pragma unroll
    for (int qi = 0; qi < 2; ++qi)
#pragma unroll
      for (int nb = 0; nb < 9; ++nb) pb[(qi * 9 + nb) * 256] = acc[nb][qi] + cb[(qi * 9 + nb) * 64];
  }
}

__global__ __launch_bounds__(256) void k3_reduce(const char* __restrict__ Pws, char* __restrict__ out) {
  const unsigned gw = blockIdx.x * 4u + (threadIdx.x >> 6), lane = threadIdx.x & 63u;
  const GAS f32x4* p = (const GAS f32x4*)(Pws + (size_t)gw * 4096u + lane * 16u);
  const f32x4 v = (p[0] + p[64]) + (p[128] + p[192]);
  const unsigned nb = gw % 9u, t1 = gw / 9u, qb16 = t1 & 7u, j = (t1 >> 3) & 3u, b = (t1 >> 5) & 7u, br = t1 >> 8;
  const unsigned q = qb16 * 16u + (lane & 15u), n = br * 576u + j * 144u + nb * 16u + 4u * (lane >> 4);
  u32x2 w = {cvtpk(v[0], v[1]), cvtpk(v[2], v[3])};
  *(GAS u32x2*)(out + ((size_t)(b * SQ + q) * 1152u + n) * 2u) = w;
}

extern "C" void kernel_launch(void* const* d_in, const int* in_sizes, int n_in, void* d_out, int out_size, void* d_ws, size_t ws_size, hipStream_t stream) {
  static bool attr = false;
  if (!attr) {
    (void)hipFuncSetAttribute((const void*)k1_qattn, hipFuncAttributeMaxDynamicSharedMemorySize, 163840);
    (void)hipFuncSetAttribute((const void*)k2_oproj, hipFuncAttributeMaxDynamicSharedMemorySize, 147456);
    attr = true;
  }
  char* ws = (char*)d_ws;
  k1_qattn<<<256, 512, 163840, stream>>>((const char*)d_in[0], (const char*)d_in[5], (const char*)d_in[1], (const char*)d_in[6], (const char*)d_in[2], (const char*)d_in[7],
                                         (const char*)d_in[3], (const char*)d_in[8], ws + WS_O);
  k2_oproj<<<256, 512, 147456, stream>>>(ws + WS_O, (const char*)d_in[4], (const char*)d_in[9], ws + WS_P);
  k3_reduce<<<1152, 256, 0, stream>>>(ws + WS_P, (char*)d_out);
}
```

```cpp
#include <hip/hip_runtime.h>
#include <stdint.h>
#include <type_traits>

#define LAS __attribute__((address_space(3)))
#define GAS __attribute__((address_space(1)))
#define DI __device__ __forceinline__
typedef __attribute__((ext_vector_type(8))) __bf16 bf16x8;
typedef __attribute__((ext_vector_type(4))) __bf16 bf16x4;
typedef __attribute__((ext_vector_type(2))) __bf16 bf16x2;
typedef __attribute__((ext_vector_type(16))) float f32x16;
typedef __attribute__((ext_vector_type(4))) float f32x4;
typedef __attribute__((ext_vector_type(2))) float f32x2;
typedef __attribute__((ext_vector_type(4))) unsigned u32x4;
typedef __attribute__((ext_vector_type(2))) unsigned u32x2;
typedef __amdgpu_buffer_rsrc_t rsrc_t;

constexpr int NB = 8, SQ = 128, DM = 576, NH = 32, SK = 512, DH = 128;
constexpr int SLOT = 16384;
constexpr size_t O_BYTES = (size_t)2 * NB * NH * SQ * DH * 2;
constexpr size_t WS_O = 0, WS_P = O_BYTES;
constexpr float CSC = 0.08838834764831845f * 1.4426950408889634f;
constexpr float THR = 8.0f;

DI unsigned cvtpk(float lo, float hi) { f32x2 v = {lo, hi}; bf16x2 b = __builtin_convertvector(v, bf16x2); return __builtin_bit_cast(unsigned, b); }
DI rsrc_t mkrsrc(const void* p, unsigned bytes) { return __builtin_amdgcn_make_buffer_rsrc((void*)p, (short)0, (int)bytes, 0x00020000); }
template <bool NT, int IMM> DI void dma16(unsigned ldsaddr, unsigned voff, rsrc_t r, unsigned soff) {
  if constexpr (NT) asm volatile("s_mov_b32 m0, %0\n\ts_nop 0\n\tbuffer_load_dwordx4 %1, %2, %3 offen offset:%c4 nt lds" ::"s"(ldsaddr), "v"(voff), "s"(r), "s"(soff), "i"(IMM) : "memory");
  else asm volatile("s_mov_b32 m0, %0\n\ts_nop 0\n\tbuffer_load_dwordx4 %1, %2, %3 offen offset:%c4 lds" ::"s"(ldsaddr), "v"(voff), "s"(r), "s"(soff), "i"(IMM) : "memory");
}
template <bool NT> DI void dma16i(int imm, unsigned ldsaddr, unsigned voff, rsrc_t r, unsigned soff) {
  switch (imm) {
    case 0: dma16<NT, 0>(ldsaddr, voff, r, soff); break;
    case 1: dma16<NT, 1024>(ldsaddr, voff, r, soff); break;
    case 2: dma16<NT, 2048>(ldsaddr, voff, r, soff); break;
    default: dma16<NT, 3072>(ldsaddr, voff, r, soff); break;
  }
}
DI unsigned opq(unsigned x) { asm volatile("" : "+s"(x)); return x; }
#define WAITV(n) asm volatile("s_waitcnt vmcnt(" #n ")" ::: "memory")
#define BARRIER() asm volatile("s_waitcnt lgkmcnt(0)\n\ts_barrier" ::: "memory")
DI bf16x8 lds_b128(unsigned a) { return *(LAS bf16x8*)(size_t)a; }
DI bf16x4 lds_tr(unsigned a) { return __builtin_amdgcn_ds_read_tr16_b64_v4bf16((LAS bf16x4*)(size_t)a); }
DI bf16x8 cat(bf16x4 a, bf16x4 b) { return __builtin_shufflevector(a, b, 0, 1, 2, 3, 4, 5, 6, 7); }
DI float swapmax(float v) { auto r = __builtin_amdgcn_permlane32_swap(__float_as_uint(v), __float_as_uint(v), false, false); return fmaxf(__uint_as_float(r[0]), __uint_as_float(r[1])); }
DI float swapsum(float v) { auto r = __builtin_amdgcn_permlane32_swap(__float_as_uint(v), __float_as_uint(v), false, false); return __uint_as_float(r[0]) + __uint_as_float(r[1]); }
#define SB() __builtin_amdgcn_sched_barrier(0)
DI float max2f(float a, float b) { return __builtin_elementwise_maximum(a, b); }
DI float max3f(float a, float b, float c) { return max2f(max2f(a, b), c); }
#define MFMA32(a, b, c) __builtin_amdgcn_mfma_f32_32x32x16_bf16(a, b, c, 0, 0, 0)
#define MFMA16(a, b, c) __builtin_amdgcn_mfma_f32_16x16x32_bf16(a, b, c, 0, 0, 0)

#define STAMP(i) do {} while (0)
#define CSTAMP(i) do {} while (0)
#define STAMP_DECL do {} while (0)
#define ISTAMP(i) do {} while (0)
#define STAMP_FLUSH(base, lo, hi) do {} while (0)
#define KV_NT true
#define W_NT false
#define LOOKAHEAD 8
#define KVSTART 1

__global__ __launch_bounds__(512, 2) void k1_qattn(const char* __restrict__ xA, const char* __restrict__ xB, const char* __restrict__ kA, const char* __restrict__ kB,
                                                   const char* __restrict__ vA, const char* __restrict__ vB, const char* __restrict__ wqA, const char* __restrict__ wqB,
                                                   char* __restrict__ Ows) {
  extern __shared__ __attribute__((aligned(16))) char lds[];
  const unsigned tid = threadIdx.x, lane = tid & 63u;
  const unsigned wid = __builtin_amdgcn_readfirstlane(tid >> 6);
  STAMP_DECL;
  STAMP(0); CSTAMP(60);
  const unsigned bi = blockIdx.x, xcd = bi & 7u, r = bi >> 3, br = xcd >> 2, hg = xcd & 3u, b = r & 7u, hp = r >> 3, h0 = hg * 8u + hp * 2u;
  const unsigned L0 = (unsigned)(size_t)lds;
  const rsrc_t rx = mkrsrc((br ? xB : xA) + (size_t)b * SQ * DM * 2, SQ * DM * 2);
  const rsrc_t rw = mkrsrc(br ? wqB : wqA, DM * NH * DH * 2);
  const rsrc_t rk = mkrsrc((br ? kB : kA) + (size_t)(b * NH + h0) * SK * DH * 2, 2 * SK * DH * 2);
  const rsrc_t rv = mkrsrc((br ? vB : vA) + (size_t)(b * NH + h0) * SK * DH * 2, 2 * SK * DH * 2);
  const unsigned l15 = lane & 15u, l31 = lane & 31u, hi = lane >> 5, g4 = lane >> 4, blk = g4 & 1u, qq = l15 >> 2, pp = lane & 3u;
  const unsigned hsel = wid >> 2, qb = wid & 3u;

  unsigned vx[2], vw[2];
#pragma unroll
  for (unsigned j = 0; j < 2; ++j) {
    vx[j] = (lane >> 3) * 1152u + (((lane & 7u) ^ (4u * j + g4)) << 4) + j * (9216u - 1024u);
    vw[j] = g4 * 8192u + ((l15 ^ (g4 << 2)) << 4) + j * (32768u - 1024u);
  }
  const unsigned vv = g4 * 256u + ((l15 ^ (g4 << 2)) << 4);
  auto vk = [&](unsigned p) __attribute__((always_inline)) { return g4 * 256u + ((l15 ^ ((4u * (p & 3u) + g4) & 15u)) << 4); };
  const unsigned vkA = vk(2u * wid), vkB = vk(2u * wid + 1u);
  unsigned w2k = wid * 2048u;
  auto kv_piece = [&](bool isV, unsigned head, unsigned t, unsigned slot, int j) __attribute__((always_inline)) {
    const unsigned so = head * 131072u + t * 16384u + w2k, la = L0 + slot * SLOT + w2k;
    if (isV) dma16i<KV_NT>(j, la, vv, rv, so);
    else dma16i<KV_NT>(j, la, j ? vkB : vkA, rk, so);
  };

  const unsigned rot = (b + ((hp * 9u + 2u) >> 2)) % 9u;
  auto stage1 = [&](int i, int j) __attribute__((always_inline)) {
    unsigned ks = rot + (unsigned)i; ks = ks >= 9u ? ks - 9u : ks;
    const unsigned buf = L0 + (unsigned)((i % 3) * 3) * SLOT + w2k;
    if (j < 2) dma16i<W_NT>(j, buf, vx[j], rx, wid * 18432u + ks * 128u);
    else { const unsigned h = (j >> 1) - 1u; dma16i<W_NT>(j & 1, buf + (1u + h) * SLOT, vw[j & 1], rw, (ks * 64u + 8u * wid) * 8192u + (h0 + h) * 256u); }
  };
  auto issueA = [&](int i, int j) __attribute__((always_inline)) {
    if (i == -1) { if (j >= 4 && j < 8) stage1(2, j - 4); return; }
    if (i <= 5) { if (j < 6) stage1(i + 3, j); }
    else if (i == 6) { if (j < 2) kv_piece(false, 0, 0, 9, j & 1); else if (j < 4) kv_piece(false, 1, 0, 1, j & 1); else if (j < 6) kv_piece(true, 0, 0, 2, j & 1); }
    else if (i == 7) { if (j < 2) kv_piece(false, 0, 1, 3, j & 1); else if (j < 4) kv_piece(true, 1, 0, 4, j & 1); }
  };
#pragma unroll
  for (int j = 0; j < 6; ++j) stage1(0, j);
  asm volatile("s_barrier" ::: "memory");
#pragma unroll
  for (int j = 0; j < 6; ++j) stage1(1, j);
  f32x16 acc[4] = {};
  const unsigned xrow = 32u * qb + l31;
  unsigned ax[4], aw[4];
#pragma unroll
  for (unsigned ks = 0; ks < 4; ++ks) ax[ks] = L0 + xrow * 128u + (((2u * ks + hi) ^ ((xrow >> 1) & 7u)) << 4);
#pragma unroll
  for (unsigned db = 0; db < 4; ++db) aw[db] = L0 + 256u * (8u * hi + qq) + (((4u * db + 2u * blk + (pp & 1u)) ^ (qq << 2)) << 4) + 8u * (pp >> 1) + (1u + hsel) * SLOT;
  {
    bf16x8 fx[2], fa[2][4];
    auto rdA = [&](int i, int ks, int db) __attribute__((always_inline)) {
      const unsigned buf = (unsigned)((i % 3) * 3) * SLOT;
      if (db == 0) fx[ks & 1] = lds_b128(buf + ax[ks]);
      fa[ks & 1][db] = cat(lds_tr(buf + aw[db] + 256u * (16u * ks)), lds_tr(buf + aw[db] + 256u * (16u * ks + 4u)));
    };
    WAITV(6);
    BARRIER();
    STAMP(1);
#pragma unroll
    for (int db = 0; db < 4; ++db) rdA(0, 0, db);
#pragma unroll
    for (int i = 0; i < 9; ++i) {
#pragma unroll
      for (int ks = 0; ks < 4; ++ks) {
        if (ks == 3 && i < 8) {
          WAITV(6);
          BARRIER();
          STAMP(2 + i);
        }
#pragma unroll
        for (int db = 0; db < 4; ++db) {
          SB();
          acc[db] = MFMA32(fa[ks & 1][db], fx[ks & 1], acc[db]);
          SB();
          if (ks < 3) rdA(i, ks + 1, db); else if (i < 8) rdA(i + 1, 0, db);
          SB();
          if (ks == 3) issueA(i, db); else if (ks == 0) issueA(i - 1, 4 + db);
          if (i == 0 && ks == 1 && db < 2) stage1(2, 4 + db);
        }
        SB();
      }
    }
  }
  bf16x8 qf[8];
#pragma unroll
  for (int s = 0; s < 8; ++s) {
    u32x4 w;
#pragma unroll
    for (int e = 0; e < 4; ++e) w[e] = cvtpk(acc[s >> 1][8 * (s & 1) + 2 * e] * CSC, acc[s >> 1][8 * (s & 1) + 2 * e + 1] * CSC);
    qf[s] = __builtin_bit_cast(bf16x8, w);
  }

  w2k = opq(wid * 2048u);
  unsigned ak[8], av[4];
#pragma unroll
  for (unsigned s = 0; s < 8; ++s) ak[s] = 256u * l31 + (((2u * s + hi) ^ l15) << 4);
#pragma unroll
  for (unsigned db = 0; db < 4; ++db) av[db] = 256u * (4u * hi + qq) + (((4u * db + 2u * blk + (pp >> 1)) ^ (qq << 2)) << 4) + 8u * (pp & 1u);
  f32x16 o[4] = {};
  float m = 0.f, l = 0.f;
  f32x16 S[2], negm = {};
  bf16x8 pf[4];
  unsigned sn = 5u;
  auto nxt = [](unsigned s_) __attribute__((always_inline)) { return s_ == 9u ? 0u : s_ + 1u; };
  unsigned s0 = 0, s1 = 0, s2 = 0, s3 = 0;
  auto slots = [&]() __attribute__((always_inline)) { s0 = sn; s1 = nxt(s0); s2 = nxt(s1); s3 = nxt(s2); sn = nxt(s3); };
  auto issueB = [&](int t, int j) __attribute__((always_inline)) {
    const int q = j >> 1;
    if (q == 0) { if (t < 7) kv_piece(false, 1, t + 1, s0, j & 1); }
    else if (q == 1) { if (t < 7) kv_piece(true, 0, t + 1, s1, j & 1); }
    else if (q == 2) { if (t < 6) kv_piece(false, 0, t + 2, s2, j & 1); }
    else { if (t < 7) kv_piece(true, 1, t + 1, s3, j & 1); }
  };
  unsigned curK = 0, curV = 0;
  auto setK = [&](unsigned kb0) __attribute__((always_inline)) {
    const unsigned d = kb0 - curK; curK = kb0;
#pragma unroll
    for (int i = 0; i < 8; ++i) { ak[i] += d; asm volatile("" : "+v"(ak[i])); }
  };
  auto setV = [&](unsigned vb0) __attribute__((always_inline)) {
    const unsigned d = vb0 - curV; curV = vb0;
#pragma unroll
    for (int i = 0; i < 4; ++i) { av[i] += d; asm volatile("" : "+v"(av[i])); }
  };
  constexpr int DL = LOOKAHEAD;
  bf16x8 kf[DL], vf[DL];
  auto rdK = [&](int f) __attribute__((always_inline)) { kf[f % DL] = lds_b128(ak[f & 7] + 8192u * (f >> 3)); };
  auto rdV = [&](int f) __attribute__((always_inline)) { vf[f % DL] = cat(lds_tr(av[f & 3] + 256u * (16u * (f >> 2))), lds_tr(av[f & 3] + 256u * (16u * (f >> 2) + 8u))); };
  auto QK = [&](int t, int j0, bool nextV) __attribute__((always_inline)) {
#pragma unroll
    for (int f = 0; f < 16; ++f) {
      SB();
      if ((f & 7) == 0) S[f >> 3] = MFMA32(kf[f % DL], qf[f & 7], negm);
      else S[f >> 3] = MFMA32(kf[f % DL], qf[f & 7], S[f >> 3]);
      SB();
      if (f + DL < 16) rdK(f + DL); else if (nextV) rdV(f + DL - 16);
      SB();
      if ((f & 3) == 1) issueB(t, j0 + (f >> 2));
    }
    SB();
  };
  auto SM = [&](bool first) __attribute__((always_inline)) {
    SB(); __builtin_amdgcn_s_setprio(2); SB();
    float pm0 = max3f(S[0][0], S[0][1], S[1][0]), pm1 = max3f(S[0][2], S[0][3], S[1][1]);
    pm0 = max3f(pm0, S[1][2], S[1][3]);
#pragma unroll
    for (int e = 4; e < 16; e += 4) {
      pm0 = max3f(pm0, S[0][e], S[0][e + 1]); pm1 = max3f(pm1, S[0][e + 2], S[0][e + 3]);
      pm0 = max3f(pm0, S[1][e], S[1][e + 1]); pm1 = max3f(pm1, S[1][e + 2], S[1][e + 3]);
    }
    float pm = max2f(pm0, pm1);
    { auto rr = __builtin_amdgcn_permlane32_swap(__float_as_uint(pm), __float_as_uint(pm), false, false); pm = max2f(__uint_as_float(rr[0]), __uint_as_float(rr[1])); }
    if (first || __builtin_expect(__any(!(pm <= THR)), 0)) {
      const float dl = first ? pm : max2f(pm, 0.f);
      m += dl;
      if (!first) {
        const float al = __builtin_amdgcn_exp2f(-dl);
        l *= al;
#pragma unroll
        for (int db = 0; db < 4; ++db)
#pragma unroll
          for (int e = 0; e < 16; ++e) o[db][e] *= al;
      }
#pragma unroll
      for (int e = 0; e < 16; ++e) { S[0][e] -= dl; S[1][e] -= dl; negm[e] = -m; }
    }
    float l0 = 0.f, l1 = 0.f;
#pragma unroll
    for (int kb = 0; kb < 2; ++kb) {
#pragma unroll
      for (int e = 0; e < 16; e += 2) {
        S[kb][e] = __builtin_amdgcn_exp2f(S[kb][e]); l0 += S[kb][e];
        S[kb][e + 1] = __builtin_amdgcn_exp2f(S[kb][e + 1]); l1 += S[kb][e + 1];
      }
#pragma unroll
      for (int u = 0; u < 2; ++u) {
        u32x4 w;
#pragma unroll
        for (int e = 0; e < 4; ++e) w[e] = cvtpk(S[kb][8 * u + 2 * e], S[kb][8 * u + 2 * e + 1]);
        pf[2 * kb + u] = __builtin_bit_cast(bf16x8, w);
      }
    }
    l += l0 + l1;
    asm volatile("" : "+v"(pf[0]), "+v"(pf[1]), "+v"(pf[2]), "+v"(pf[3]), "+v"(l));
    SB(); __builtin_amdgcn_s_setprio(0); SB();
  };
  auto waitB = [&](int t) __attribute__((always_inline)) { if (t == 8) WAITV(0); else if (t == 7) WAITV(2); else WAITV(4); BARRIER(); };
  if (hsel == 0) {
    unsigned sK = 9u, sV = 2u;
    auto stepA = [&](int t, auto firstc) __attribute__((always_inline)) {
      constexpr bool FIRST = decltype(firstc)::value;
      waitB(t);
      STAMP(11 + t);
      ISTAMP(47);
      slots();
      setK(L0 + sK * SLOT);
#pragma unroll
      for (int i = 0; i < 4; ++i) {
        SB();
#pragma unroll
        for (int f = i * DL / 4; f < (i + 1) * DL / 4; ++f) rdK(f);
        SB();
        if (!FIRST) o[i] = MFMA32(vf[(12 + i) % DL], pf[3], o[i]);
      }
      SB();
      setV(L0 + sV * SLOT);
      ISTAMP(43);
      QK(t, 0, true);
      ISTAMP(44);
      SM(FIRST);
      ISTAMP(45);
#pragma unroll
      for (int f = 0; f < 12; ++f) {
        SB();
        o[f & 3] = MFMA32(vf[f % DL], pf[f >> 2], o[f & 3]);
        SB();
        if (f + DL < 16) rdV(f + DL);
        SB();
        if ((f & 3) == 1) issueB(t, 4 + (f >> 2));
        if (f == 11) issueB(t, 7);
      }
      SB();
      ISTAMP(46);
      sK += 4u; if (sK >= 10u) sK -= 10u;
      sV += 4u; if (sV >= 10u) sV -= 10u;
    };
    stepA(0, std::true_type{});
#pragma unroll 1
    for (int t = 1; t < 8; ++t) stepA(t, std::false_type{});
    waitB(8);
    STAMP(19);
#pragma unroll
    for (int i = 0; i < 4; ++i) o[i] = MFMA32(vf[(12 + i) % DL], pf[3], o[i]);
  } else {
    unsigned sK = 1u, sV = 0u;
    auto stepB = [&](int t, auto firstc, auto lastc) __attribute__((always_inline)) {
      constexpr bool FIRST = decltype(firstc)::value, LAST = decltype(lastc)::value;
      waitB(t);
      STAMP(11 + t);
      if (!LAST) ISTAMP(47);
      slots();
      setV(L0 + sV * SLOT);
#pragma unroll
      for (int f = 0; f < DL; ++f) rdV(f);
      if (!LAST) setK(L0 + sK * SLOT);
      SB();
      if (!LAST) ISTAMP(43);
      SM(FIRST);
      if (!LAST) ISTAMP(44);
#pragma unroll
      for (int f = 0; f < 16; ++f) {
        SB();
        o[f & 3] = MFMA32(vf[f % DL], pf[f >> 2], o[f & 3]);
        SB();
        if (f + DL < 16) rdV(f + DL); else if (!LAST) rdK(f + DL - 16);
        SB();
        if ((f & 3) == 1 && !LAST) issueB(t, f >> 2);
      }
      if (!LAST) ISTAMP(45);
      if (!LAST) QK(t, 4, false);
      SB();
      if (!LAST) ISTAMP(46);
      sK += 4u; if (sK >= 10u) sK -= 10u;
      sV += 4u; if (sV >= 10u) sV -= 10u;
    };
    {
      waitB(0);
      STAMP(11);
      slots();
      setK(L0 + sK * SLOT);
#pragma unroll
      for (int f = 0; f < DL; ++f) rdK(f);
#pragma unroll
      for (int j = 0; j < 4; ++j) issueB(0, j);
      QK(0, 4, false);
      sK = 5u; sV = 4u;
    }
    stepB(1, std::true_type{}, std::false_type{});
#pragma unroll 1
    for (int t = 2; t < 8; ++t) stepB(t, std::false_type{}, std::false_type{});
    stepB(8, std::false_type{}, std::true_type{});
  }
  STAMP(27);
  l = swapsum(l);
  const float inv = __builtin_amdgcn_rcpf(l);
  char* ob = Ows + ((((size_t)(br * NB + b) * NH + (h0 + hsel)) * 8u + (2u * qb + (l31 >> 4))) * 4u) * 1024u + (l15 + 16u * hi) * 16u;
#pragma unroll
  for (int db = 0; db < 4; ++db)
#pragma unroll
    for (int u = 0; u < 2; ++u) {
      u32x4 w;
#pragma unroll
      for (int e = 0; e < 4; ++e) w[e] = cvtpk(o[db][8 * u + 2 * e] * inv, o[db][8 * u + 2 * e + 1] * inv);
      *(GAS u32x4*)(ob + db * 1024 + u * 512) = w;
    }
  STAMP(28); CSTAMP(61); STAMP_FLUSH(Ows, 0u, 32u);
}

__global__ __launch_bounds__(512, 2) void k2_oproj(const char* __restrict__ Ows, const char* __restrict__ woA, const char* __restrict__ woB, char* __restrict__ Pws) {
  extern __shared__ __attribute__((aligned(16))) char lds[];
  const unsigned tid = threadIdx.x, lane = tid & 63u;
  const unsigned wid = __builtin_amdgcn_readfirstlane(tid >> 6);
  STAMP_DECL;
  STAMP(32); CSTAMP(62);
  const unsigned bi = blockIdx.x, xcd = bi & 7u, r = bi >> 3, br = xcd >> 2, hg = xcd & 3u, b = r & 7u, j = r >> 3;
  const unsigned L0 = (unsigned)(size_t)lds;
  const unsigned qb = wid & 3u, kh = wid >> 2;
  const rsrc_t rw = mkrsrc(br ? woB : woA, NH * DH * DM * 2);
  const rsrc_t ro = mkrsrc(Ows + (size_t)(br * NB + b) * NH * 32768u, NH * 32768u);
  unsigned vw[5];
#pragma unroll
  for (unsigned i = 0; i < 5; ++i) { const unsigned c = 64u * (qb + 4u * i) + lane; vw[i] = (c / 18u) * 1152u + (c % 18u) * 16u; }
  auto kpos = [&](int u) __attribute__((always_inline)) { return ((unsigned)u + b) & 7u; };
  const bool lowq = qb < 2u;
  const unsigned ldst = L0 + kh * 73728u + qb * 1024u;
  auto stage1 = [&](int u, int i) __attribute__((always_inline)) {
    if (i == 4 && !lowq) return;
    const unsigned so = ((hg * 8u + kh * 4u) * 128u + kpos(u) * 64u) * 1152u + j * 288u;
    dma16<W_NT, 0>(ldst + (unsigned)(u & 3) * 18432u + (unsigned)i * 4096u, vw[i], rw, so);
  };
  const unsigned vo0 = lane * 16u + 2u * qb * 4096u, vo1 = vo0 + 4096u;
  bf16x8 bf[4][4];
  auto loadB = [&](int u, int f) __attribute__((always_inline)) {
    const unsigned kp = kpos(u), so = (hg * 8u + kh * 4u + (kp >> 1)) * 32768u + 2u * (kp & 1u) * 1024u;
    if (f == 0) asm volatile("buffer_load_dwordx4 %0, %1, %2, %3 offen" : "=v"(bf[u & 3][0]) : "v"(vo0), "s"(ro), "s"(so) : "memory");
    if (f == 1) asm volatile("buffer_load_dwordx4 %0, %1, %2, %3 offen" : "=v"(bf[u & 3][1]) : "v"(vo1), "s"(ro), "s"(so) : "memory");
    if (f == 2) asm volatile("buffer_load_dwordx4 %0, %1, %2, %3 offen offset:1024" : "=v"(bf[u & 3][2]) : "v"(vo0), "s"(ro), "s"(so) : "memory");
    if (f == 3) asm volatile("buffer_load_dwordx4 %0, %1, %2, %3 offen offset:1024" : "=v"(bf[u & 3][3]) : "v"(vo1), "s"(ro), "s"(so) : "memory");
  };
  auto issue = [&](int u, int v) __attribute__((always_inline)) {
    if (u > 7) return;
    if (v < 5) stage1(u, v); else if (v < 9) loadB(u, v - 5);
  };
  const unsigned aw = L0 + 288u * (16u * (lane >> 5) + 4u * ((lane >> 4) & 1u) + ((lane & 15u) >> 2)) + 8u * (lane & 3u) + kh * 73728u;
  f32x4 acc[9][2] = {};
  bf16x8 fa[2][3];
  auto rdW = [&](int u, int g, int i) __attribute__((always_inline)) {
    const int e = g / 3, n0 = 3 * (g % 3);
    const unsigned buf = aw + (unsigned)(u & 3) * 18432u + (unsigned)e * 9216u;
    fa[g & 1][i] = cat(lds_tr(buf + 32u * (n0 + i)), lds_tr(buf + 288u * 8u + 32u * (n0 + i)));
  };
  auto waitK2 = [&](int u, int N) __attribute__((always_inline)) {
    bf16x8* c = bf[u & 3];
    if (N == 0) WAITV(0);
    else if (N == 1) { if (lowq) WAITV(9); else WAITV(8); }
    else { if (lowq) WAITV(18); else WAITV(16); }
    asm volatile("" : "+v"(c[0]), "+v"(c[1]), "+v"(c[2]), "+v"(c[3]) :: "memory");
    BARRIER();
  };
#pragma unroll
  for (int v = 0; v < 9; ++v) issue(0, v);
  asm volatile("s_barrier" ::: "memory");
#pragma unroll
  for (int v = 0; v < 9; ++v) issue(1, v);
  waitK2(0, 1);
  STAMP(33);
#pragma unroll
  for (int i = 0; i < 3; ++i) rdW(0, 0, i);
#pragma unroll
  for (int u = 0; u < 8; ++u) {
    bf16x8* cur = bf[u & 3];
#pragma unroll
    for (int g = 0; g < 6; ++g) {
      if (g == 5 && u < 7) { waitK2(u + 1, u + 2 <= 7 ? 1 : 0); STAMP(34 + u); }
      const int e = g / 3, n0 = 3 * (g % 3);
#pragma unroll
      for (int i = 0; i < 3; ++i) {
        SB();
        acc[n0 + i][0] = MFMA16(fa[g & 1][i], cur[2 * e], acc[n0 + i][0]);
        acc[n0 + i][1] = MFMA16(fa[g & 1][i], cur[2 * e + 1], acc[n0 + i][1]);
        SB();
        if (g < 5) rdW(u, g + 1, i); else if (u < 7) rdW(u + 1, 0, i);
        SB();
        const int v = 3 * g + i;
        if (u == 0 && v < 9) issue(2, v);
        if (v >= 15) issue(u + 3, v - 15);
        if (u > 0 && v < 6) issue(u + 2, 3 + v);
      }
    }
    SB();
  }
  BARRIER();
  STAMP(41);
  LAS f32x4* cb = (LAS f32x4*)(size_t)(L0 + qb * 18432u + lane * 16u);
  if (kh == 1) {
#pragma unroll
    for (int nb = 0; nb < 9; ++nb) { cb[(0 * 9 + nb) * 64] = acc[nb][0]; cb[(1 * 9 + nb) * 64] = acc[nb][1]; }
  }
  BARRIER();
  if (kh == 0) {
    GAS f32x4* pb = (GAS f32x4*)(Pws + ((((size_t)(br * NB + b) * 4u + j) * 8u + 2u * qb) * 9u * 4u + hg) * 1024u + lane * 16u);
#pragma unroll
    for (int qi = 0; qi < 2; ++qi)
#pragma unroll
      for (int nb = 0; nb < 9; ++nb) pb[(qi * 9 + nb) * 256] = acc[nb][qi] + cb[(qi * 9 + nb) * 64];
  }
  STAMP(42); CSTAMP(63); STAMP_FLUSH((char*)Ows, 32u, 43u);
}

__global__ __launch_bounds__(256) void k3_reduce(const char* __restrict__ Pws, char* __restrict__ out) {
  const unsigned gw = blockIdx.x * 4u + (threadIdx.x >> 6), lane = threadIdx.x & 63u;
  const GAS f32x4* p = (const GAS f32x4*)(Pws + (size_t)gw * 4096u + lane * 16u);
  const f32x4 v = (p[0] + p[64]) + (p[128] + p[192]);
  const unsigned nb = gw % 9u, t1 = gw / 9u, qb16 = t1 & 7u, j = (t1 >> 3) & 3u, b = (t1 >> 5) & 7u, br = t1 >> 8;
  const unsigned q = qb16 * 16u + (lane & 15u), n = br * 576u + j * 144u + nb * 16u + 4u * (lane >> 4);
  u32x2 w = {cvtpk(v[0], v[1]), cvtpk(v[2], v[3])};
  *(GAS u32x2*)(out + ((size_t)(b * SQ + q) * 1152u + n) * 2u) = w;
}

extern "C" void kernel_launch(void* const* d_in, const int* in_sizes, int n_in, void* d_out, int out_size, void* d_ws, size_t ws_size, hipStream_t stream) {
  static bool attr = false;
  if (!attr) {
    (void)hipFuncSetAttribute((const void*)k1_qattn, hipFuncAttributeMaxDynamicSharedMemorySize, 163840);
    (void)hipFuncSetAttribute((const void*)k2_oproj, hipFuncAttributeMaxDynamicSharedMemorySize, 147456);
    attr = true;
  }
  char* ws = (char*)d_ws;
  k1_qattn<<<256, 512, 163840, stream>>>((const char*)d_in[0], (const char*)d_in[5], (const char*)d_in[1], (const char*)d_in[6], (const char*)d_in[2], (const char*)d_in[7],
                                         (const char*)d_in[3], (const char*)d_in[8], ws + WS_O);
  k2_oproj<<<256, 512, 147456, stream>>>(ws + WS_O, (const char*)d_in[4], (const char*)d_in[9], ws + WS_P);
  k3_reduce<<<1152, 256, 0, stream>>>(ws + WS_P, (char*)d_out);
}
```

```cpp
#include <hip/hip_runtime.h>
#include <stdint.h>
#include <type_traits>
#include <utility>

#define LAS __attribute__((address_space(3)))
#define GAS __attribute__((address_space(1)))
#define DI __device__ __forceinline__
typedef __attribute__((ext_vector_type(8))) __bf16 bf16x8;
typedef __attribute__((ext_vector_type(4))) __bf16 bf16x4;
typedef __attribute__((ext_vector_type(2))) __bf16 bf16x2;
typedef __attribute__((ext_vector_type(16))) float f32x16;
typedef __attribute__((ext_vector_type(4))) float f32x4;
typedef __attribute__((ext_vector_type(2))) float f32x2;
typedef __attribute__((ext_vector_type(4))) unsigned u32x4;
typedef __attribute__((ext_vector_type(2))) unsigned u32x2;
typedef __amdgpu_buffer_rsrc_t rsrc_t;

constexpr int NB = 8, SQ = 128, DM = 576, NH = 32, SK = 512, DH = 128;
constexpr int SLOT = 16384;
constexpr size_t O_BYTES = (size_t)2 * NB * NH * SQ * DH * 2;
constexpr size_t WS_O = 0, WS_P = O_BYTES;
constexpr size_t P_BYTES = (size_t)64 * 4 * 4 * 18432, WS_C = WS_P + P_BYTES;
constexpr float CSC = 0.08838834764831845f * 1.4426950408889634f;
constexpr float THR = 8.0f;

DI unsigned cvtpk(float lo, float hi) { f32x2 v = {lo, hi}; bf16x2 b = __builtin_convertvector(v, bf16x2); return __builtin_bit_cast(unsigned, b); }
DI rsrc_t mkrsrc(const void* p, unsigned bytes) { return __builtin_amdgcn_make_buffer_rsrc((void*)p, (short)0, (int)bytes, 0x00020000); }
template <bool NT, int IMM> DI void dma16(unsigned ldsaddr, unsigned voff, rsrc_t r, unsigned soff) {
  if constexpr (NT) asm volatile("s_mov_b32 m0, %0\n\ts_nop 0\n\tbuffer_load_dwordx4 %1, %2, %3 offen offset:%c4 nt lds" ::"s"(ldsaddr), "v"(voff), "s"(r), "s"(soff), "i"(IMM) : "memory");
  else asm volatile("s_mov_b32 m0, %0\n\ts_nop 0\n\tbuffer_load_dwordx4 %1, %2, %3 offen offset:%c4 lds" ::"s"(ldsaddr), "v"(voff), "s"(r), "s"(soff), "i"(IMM) : "memory");
}
template <bool NT> DI void dma16i(int imm, unsigned ldsaddr, unsigned voff, rsrc_t r, unsigned soff) {
  switch (imm) {
    case 0: dma16<NT, 0>(ldsaddr, voff, r, soff); break;
    case 1: dma16<NT, 1024>(ldsaddr, voff, r, soff); break;
    case 2: dma16<NT, 2048>(ldsaddr, voff, r, soff); break;
    default: dma16<NT, 3072>(ldsaddr, voff, r, soff); break;
  }
}
template <int... I, class F> DI void sfor_impl(std::integer_sequence<int, I...>, F&& f) { (f(std::integral_constant<int, I>{}), ...); }
template <int N, class F> DI void sfor(F&& f) { sfor_impl(std::make_integer_sequence<int, N>{}, f); }
DI unsigned opq(unsigned x) { asm volatile("" : "+s"(x)); return x; }
#define WAITV(n) asm volatile("s_waitcnt vmcnt(" #n ")" ::: "memory")
#define BARRIER() asm volatile("s_waitcnt lgkmcnt(0)\n\ts_barrier" ::: "memory")
DI bf16x8 lds_b128(unsigned a) { return *(LAS bf16x8*)(size_t)a; }
DI bf16x4 lds_tr(unsigned a) { return __builtin_amdgcn_ds_read_tr16_b64_v4bf16((LAS bf16x4*)(size_t)a); }
DI bf16x8 cat(bf16x4 a, bf16x4 b) { return __builtin_shufflevector(a, b, 0, 1, 2, 3, 4, 5, 6, 7); }
DI float swapmax(float v) { auto r = __builtin_amdgcn_permlane32_swap(__float_as_uint(v), __float_as_uint(v), false, false); return fmaxf(__uint_as_float(r[0]), __uint_as_float(r[1])); }
DI float swapsum(float v) { auto r = __builtin_amdgcn_permlane32_swap(__float_as_uint(v), __float_as_uint(v), false, false); return __uint_as_float(r[0]) + __uint_as_float(r[1]); }
#define SB() __builtin_amdgcn_sched_barrier(0)
DI float max2f(float a, float b) { return __builtin_elementwise_maximum(a, b); }
DI float max3f(float a, float b, float c) { return max2f(max2f(a, b), c); }
#define MFMA32(a, b, c) __builtin_amdgcn_mfma_f32_32x32x16_bf16(a, b, c, 0, 0, 0)
#define MFMA16(a, b, c) __builtin_amdgcn_mfma_f32_16x16x32_bf16(a, b, c, 0, 0, 0)

#define STAMP(i) do {} while (0)
#define CSTAMP(i) do {} while (0)
#define STAMP_DECL do {} while (0)
#define ISTAMP(i) do {} while (0)
#define STAMP_FLUSH(base, lo, hi) do {} while (0)
#define KV_NT true
#define W_NT false
#define LOOKAHEAD 8
#define W2_NT false
#define KVSTART 1

__global__ __launch_bounds__(512, 2) void k1_qattn(const char* __restrict__ xA, const char* __restrict__ xB, const char* __restrict__ kA, const char* __restrict__ kB,
                                                   const char* __restrict__ vA, const char* __restrict__ vB, const char* __restrict__ wqA, const char* __restrict__ wqB,
                                                   const char* __restrict__ woA, const char* __restrict__ woB, char* __restrict__ Ows) {
  extern __shared__ __attribute__((aligned(16))) char lds[];
  const unsigned tid = threadIdx.x, lane = tid & 63u;
  const unsigned wid = __builtin_amdgcn_readfirstlane(tid >> 6);
  STAMP_DECL;
  STAMP(0); CSTAMP(60);
  const unsigned bi = blockIdx.x, xcd = bi & 7u, r = bi >> 3, br = xcd >> 2, hg = xcd & 3u, b = r & 7u, hp = r >> 3, h0 = hg * 8u + hp * 2u;
  const unsigned L0 = (unsigned)(size_t)lds;
  const rsrc_t rx = mkrsrc((br ? xB : xA) + (size_t)b * SQ * DM * 2, SQ * DM * 2);
  const rsrc_t rw = mkrsrc(br ? wqB : wqA, DM * NH * DH * 2);
  const rsrc_t rk = mkrsrc((br ? kB : kA) + (size_t)(b * NH + h0) * SK * DH * 2, 2 * SK * DH * 2);
  const rsrc_t rv = mkrsrc((br ? vB : vA) + (size_t)(b * NH + h0) * SK * DH * 2, 2 * SK * DH * 2);
  if (tid == 0) *(GAS unsigned*)(Ows + WS_C + (size_t)(((br * NB + b) * 4u + hp) * 4u + hg) * 128u) = 0u;
  const unsigned l15 = lane & 15u, l31 = lane & 31u, hi = lane >> 5, g4 = lane >> 4, blk = g4 & 1u, qq = l15 >> 2, pp = lane & 3u;
  const unsigned hsel = wid >> 2, qb = wid & 3u;

  unsigned vx[2], vw[2];
#pragma unroll
  for (unsigned j = 0; j < 2; ++j) {
    vx[j] = (lane >> 3) * 1152u + (((lane & 7u) ^ (4u * j + g4)) << 4) + j * (9216u - 1024u);
    vw[j] = g4 * 8192u + ((l15 ^ (g4 << 2)) << 4) + j * (32768u - 1024u);
  }
  const unsigned vv = g4 * 256u + ((l15 ^ (g4 << 2)) << 4);
  auto vk = [&](unsigned p) __attribute__((always_inline)) { return g4 * 256u + ((l15 ^ ((4u * (p & 3u) + g4) & 15u)) << 4); };
  const unsigned vkA = vk(2u * wid), vkB = vk(2u * wid + 1u);
  unsigned w2k = wid * 2048u;
  auto kv_piece = [&](bool isV, unsigned head, unsigned t, unsigned slot, int j) __attribute__((always_inline)) {
    const unsigned so = head * 131072u + t * 16384u + w2k, la = L0 + slot * SLOT + w2k;
    if (isV) dma16i<KV_NT>(j, la, vv, rv, so);
    else dma16i<KV_NT>(j, la, j ? vkB : vkA, rk, so);
  };

  const unsigned rot = b + 2u * hp + 7u;
  auto stage1 = [&](int i, int j) __attribute__((always_inline)) {
    const unsigned ks = i == 0 ? 0u : 1u + ((rot + (unsigned)i) & 7u);
    const unsigned buf = L0 + (unsigned)((i % 3) * 3) * SLOT + w2k;
    if (j < 2) dma16i<W_NT>(j, buf, vx[j], rx, wid * 18432u + ks * 128u);
    else { const unsigned h = (j >> 1) - 1u; dma16i<W_NT>(j & 1, buf + (1u + h) * SLOT, vw[j & 1], rw, (ks * 64u + 8u * wid) * 8192u + (h0 + h) * 256u); }
  };
  auto issueA = [&](int i, int j) __attribute__((always_inline)) {
    if (i == -1) { if (j >= 4 && j < 8) stage1(2, j - 4); return; }
    if (i <= 5) { if (j < 6) stage1(i + 3, j); }
  };
  STAMP(48);
#pragma unroll
  for (int j = 0; j < 6; ++j) stage1(0, j);
  STAMP(49);
  asm volatile("s_barrier" ::: "memory");
#pragma unroll
  for (int j = 0; j < 6; ++j) stage1(1, j);
  STAMP(50);
  f32x16 acc[4] = {};
  const unsigned xrow = 32u * qb + l31;
  unsigned ax[4], aw[4];
#pragma unroll
  for (unsigned ks = 0; ks < 4; ++ks) ax[ks] = L0 + xrow * 128u + (((2u * ks + hi) ^ ((xrow >> 1) & 7u)) << 4);
#pragma unroll
  for (unsigned db = 0; db < 4; ++db) aw[db] = L0 + 256u * (8u * hi + qq) + (((4u * db + 2u * blk + (pp & 1u)) ^ (qq << 2)) << 4) + 8u * (pp >> 1) + (1u + hsel) * SLOT;
  {
    bf16x8 fx[2], fa[2][4];
    auto rdA = [&](int i, int ks, int db) __attribute__((always_inline)) {
      const unsigned buf = (unsigned)((i % 3) * 3) * SLOT;
      if (db == 0) fx[ks & 1] = lds_b128(buf + ax[ks]);
      fa[ks & 1][db] = cat(lds_tr(buf + aw[db] + 256u * (16u * ks)), lds_tr(buf + aw[db] + 256u * (16u * ks + 4u)));
    };
    WAITV(6);
    BARRIER();
    STAMP(1); CSTAMP(54);
#pragma unroll
    for (int db = 0; db < 4; ++db) rdA(0, 0, db);
#pragma unroll
    for (int i = 0; i < 9; ++i) {
#pragma unroll
      for (int ks = 0; ks < 4; ++ks) {
        if (ks == 3 && i < 8) {
          WAITV(6);
          BARRIER();
          STAMP(2 + i); if (i == 7) CSTAMP(55);
        }
#pragma unroll
        for (int db = 0; db < 4; ++db) {
          SB();
          acc[db] = MFMA32(fa[ks & 1][db], fx[ks & 1], acc[db]);
          SB();
          if (ks < 3) rdA(i, ks + 1, db); else if (i < 8) rdA(i + 1, 0, db);
          SB();
          if (ks == 3) issueA(i, db); else if (ks == 0) issueA(i - 1, 4 + db);
          if (i == 0 && ks == 1 && db < 2) stage1(2, 4 + db);
          if (i == 6 && ks == 3) { if (db < 2) kv_piece(false, 0, 0, 9, db); else kv_piece(false, 1, 0, 1, db - 2); }
          if (i == 7 && ks == 0 && db < 2) kv_piece(true, 0, 0, 2, db);
          if (i == 7 && ks == 3) { if (db < 2) kv_piece(false, 0, 1, 3, db); else kv_piece(true, 1, 0, 4, db - 2); }
        }
        SB();
      }
    }
  }
  bf16x8 qf[8];
#pragma unroll
  for (int s = 0; s < 8; ++s) {
    u32x4 w;
#pragma unroll
    for (int e = 0; e < 4; ++e) w[e] = cvtpk(acc[s >> 1][8 * (s & 1) + 2 * e] * CSC, acc[s >> 1][8 * (s & 1) + 2 * e + 1] * CSC);
    qf[s] = __builtin_bit_cast(bf16x8, w);
  }

  STAMP(10); CSTAMP(56);
  w2k = opq(wid * 2048u);
  unsigned ak[8], av[4];
#pragma unroll
  for (unsigned s = 0; s < 8; ++s) ak[s] = 256u * l31 + (((2u * s + hi) ^ l15) << 4);
#pragma unroll
  for (unsigned db = 0; db < 4; ++db) av[db] = 256u * (4u * hi + qq) + (((4u * db + 2u * blk + (pp >> 1)) ^ (qq << 2)) << 4) + 8u * (pp & 1u);
  f32x16 o[4] = {};
  float m = 0.f, l = 0.f;
  f32x16 S[2], negm = {};
  bf16x8 pf[4];
  unsigned sn = 5u;
  auto nxt = [](unsigned s_) __attribute__((always_inline)) { return s_ == 9u ? 0u : s_ + 1u; };
  unsigned s0 = 0, s1 = 0, s2 = 0, s3 = 0;
  auto slots = [&]() __attribute__((always_inline)) { s0 = sn; s1 = nxt(s0); s2 = nxt(s1); s3 = nxt(s2); sn = nxt(s3); };
  const rsrc_t rwo = mkrsrc((br ? woB : woA) + (size_t)hg * 1024u * 1152u, 1024u * 1152u);
  auto warmWo = [&]() __attribute__((always_inline)) {
#pragma unroll
    for (int i = 0; i < 4; ++i) dma16i<false>(i, L0 + s0 * SLOT + w2k, lane * 16u, rwo, r * 36864u + wid * 5120u);
    dma16<false, 0>(L0 + s1 * SLOT + w2k, lane * 16u, rwo, r * 36864u + wid * 5120u + 4096u);
  };
  auto issueB = [&](int t, int j) __attribute__((always_inline)) {
    const int q = j >> 1;
    if (j == 0 && t == 7) warmWo();
    if (q == 0) { if (t < 7) kv_piece(false, 1, t + 1, s0, j & 1); }
    else if (q == 1) { if (t < 7) kv_piece(true, 0, t + 1, s1, j & 1); }
    else if (q == 2) { if (t < 6) kv_piece(false, 0, t + 2, s2, j & 1); }
    else { if (t < 7) kv_piece(true, 1, t + 1, s3, j & 1); }
  };
  unsigned curK = 0, curV = 0;
  auto setK = [&](unsigned kb0) __attribute__((always_inline)) {
    const unsigned d = kb0 - curK; curK = kb0;
#pragma unroll
    for (int i = 0; i < 8; ++i) { ak[i] += d; asm volatile("" : "+v"(ak[i])); }
  };
  auto setV = [&](unsigned vb0) __attribute__((always_inline)) {
    const unsigned d = vb0 - curV; curV = vb0;
#pragma unroll
    for (int i = 0; i < 4; ++i) { av[i] += d; asm volatile("" : "+v"(av[i])); }
  };
  constexpr int DL = LOOKAHEAD;
  bf16x8 kf[DL], vf[DL];
  auto rdK = [&](int f) __attribute__((always_inline)) { kf[f % DL] = lds_b128(ak[f & 7] + 8192u * (f >> 3)); };
  auto rdV = [&](int f) __attribute__((always_inline)) { vf[f % DL] = cat(lds_tr(av[f & 3] + 256u * (16u * (f >> 2))), lds_tr(av[f & 3] + 256u * (16u * (f >> 2) + 8u))); };
  auto QK = [&](int t, int j0, bool nextV) __attribute__((always_inline)) {
#pragma unroll
    for (int f = 0; f < 16; ++f) {
      SB();
      if ((f & 7) == 0) S[f >> 3] = MFMA32(kf[f % DL], qf[f & 7], negm);
      else S[f >> 3] = MFMA32(kf[f % DL], qf[f & 7], S[f >> 3]);
      SB();
      if (f + DL < 16) rdK(f + DL); else if (nextV) rdV(f + DL - 16);
      SB();
      if ((f & 3) == 1) issueB(t, j0 + (f >> 2));
    }
    SB();
  };
  auto SM = [&](bool first) __attribute__((always_inline)) {
    SB(); __builtin_amdgcn_s_setprio(2); SB();
    float pm0 = max3f(S[0][0], S[0][1], S[1][0]), pm1 = max3f(S[0][2], S[0][3], S[1][1]);
    pm0 = max3f(pm0, S[1][2], S[1][3]);
#pragma unroll
    for (int e = 4; e < 16; e += 4) {
      pm0 = max3f(pm0, S[0][e], S[0][e + 1]); pm1 = max3f(pm1, S[0][e + 2], S[0][e + 3]);
      pm0 = max3f(pm0, S[1][e], S[1][e + 1]); pm1 = max3f(pm1, S[1][e + 2], S[1][e + 3]);
    }
    float pm = max2f(pm0, pm1);
    { auto rr = __builtin_amdgcn_permlane32_swap(__float_as_uint(pm), __float_as_uint(pm), false, false); pm = max2f(__uint_as_float(rr[0]), __uint_as_float(rr[1])); }
    if (first || __builtin_expect(__any(!(pm <= THR)), 0)) {
      const float dl = first ? pm : max2f(pm, 0.f);
      m += dl;
      if (!first) {
        const float al = __builtin_amdgcn_exp2f(-dl);
        l *= al;
#pragma unroll
        for (int db = 0; db < 4; ++db)
#pragma unroll
          for (int e = 0; e < 16; ++e) o[db][e] *= al;
      }
#pragma unroll
      for (int e = 0; e < 16; ++e) { S[0][e] -= dl; S[1][e] -= dl; negm[e] = -m; }
    }
    float l0 = 0.f, l1 = 0.f;
#pragma unroll
    for (int kb = 0; kb < 2; ++kb) {
#pragma unroll
      for (int e = 0; e < 16; e += 2) {
        S[kb][e] = __builtin_amdgcn_exp2f(S[kb][e]); l0 += S[kb][e];
        S[kb][e + 1] = __builtin_amdgcn_exp2f(S[kb][e + 1]); l1 += S[kb][e + 1];
      }
#pragma unroll
      for (int u = 0; u < 2; ++u) {
        u32x4 w;
#pragma unroll
        for (int e = 0; e < 4; ++e) w[e] = cvtpk(S[kb][8 * u + 2 * e], S[kb][8 * u + 2 * e + 1]);
        pf[2 * kb + u] = __builtin_bit_cast(bf16x8, w);
      }
    }
    l += l0 + l1;
    asm volatile("" : "+v"(pf[0]), "+v"(pf[1]), "+v"(pf[2]), "+v"(pf[3]), "+v"(l));
    SB(); __builtin_amdgcn_s_setprio(0); SB();
  };
  auto waitB = [&](int t) __attribute__((always_inline)) { if (t == 8) WAITV(5); else if (t == 7) WAITV(2); else WAITV(4); BARRIER(); };
  if (hsel == 0) {
    unsigned sK = 9u, sV = 2u;
    auto stepA = [&](int t, auto firstc) __attribute__((always_inline)) {
      constexpr bool FIRST = decltype(firstc)::value;
      waitB(t);
      STAMP(11 + t);
      ISTAMP(47);
      slots();
      setK(L0 + sK * SLOT);
#pragma unroll
      for (int i = 0; i < 4; ++i) {
        SB();
#pragma unroll
        for (int f = i * DL / 4; f < (i + 1) * DL / 4; ++f) rdK(f);
        SB();
        if (!FIRST) o[i] = MFMA32(vf[(12 + i) % DL], pf[3], o[i]);
      }
      SB();
      setV(L0 + sV * SLOT);
      ISTAMP(43);
      QK(t, 0, true);
      ISTAMP(44);
      SM(FIRST);
      ISTAMP(45);
#pragma unroll
      for (int f = 0; f < 12; ++f) {
        SB();
        o[f & 3] = MFMA32(vf[f % DL], pf[f >> 2], o[f & 3]);
        SB();
        if (f + DL < 16) rdV(f + DL);
        SB();
        if ((f & 3) == 1) issueB(t, 4 + (f >> 2));
        if (f == 11) issueB(t, 7);
      }
      SB();
      ISTAMP(46);
      sK += 4u; if (sK >= 10u) sK -= 10u;
      sV += 4u; if (sV >= 10u) sV -= 10u;
    };
    stepA(0, std::true_type{});
#pragma unroll 1
    for (int t = 1; t < 8; ++t) stepA(t, std::false_type{});
    waitB(8);
    STAMP(19);
#pragma unroll
    for (int i = 0; i < 4; ++i) o[i] = MFMA32(vf[(12 + i) % DL], pf[3], o[i]);
  } else {
    unsigned sK = 1u, sV = 0u;
    auto stepB = [&](int t, auto firstc, auto lastc) __attribute__((always_inline)) {
      constexpr bool FIRST = decltype(firstc)::value, LAST = decltype(lastc)::value;
      waitB(t);
      STAMP(11 + t);
      if (!LAST) ISTAMP(47);
      slots();
      setV(L0 + sV * SLOT);
#pragma unroll
      for (int f = 0; f < DL; ++f) rdV(f);
      if (!LAST) setK(L0 + sK * SLOT);
      SB();
      if (!LAST) ISTAMP(43);
      SM(FIRST);
      if (!LAST) ISTAMP(44);
#pragma unroll
      for (int f = 0; f < 16; ++f) {
        SB();
        o[f & 3] = MFMA32(vf[f % DL], pf[f >> 2], o[f & 3]);
        SB();
        if (f + DL < 16) rdV(f + DL); else if (!LAST) rdK(f + DL - 16);
        SB();
        if ((f & 3) == 1 && !LAST) issueB(t, f >> 2);
      }
      if (!LAST) ISTAMP(45);
      if (!LAST) QK(t, 4, false);
      SB();
      if (!LAST) ISTAMP(46);
      sK += 4u; if (sK >= 10u) sK -= 10u;
      sV += 4u; if (sV >= 10u) sV -= 10u;
    };
    {
      waitB(0);
      STAMP(11);
      slots();
      setK(L0 + sK * SLOT);
#pragma unroll
      for (int f = 0; f < DL; ++f) rdK(f);
#pragma unroll
      for (int j = 0; j < 4; ++j) issueB(0, j);
      QK(0, 4, false);
      sK = 5u; sV = 4u;
    }
    stepB(1, std::true_type{}, std::false_type{});
#pragma unroll 1
    for (int t = 2; t < 8; ++t) stepB(t, std::false_type{}, std::false_type{});
    stepB(8, std::false_type{}, std::true_type{});
  }
  STAMP(27); CSTAMP(57);
  l = swapsum(l);
  const float inv = __builtin_amdgcn_rcpf(l);
  const rsrc_t rob = mkrsrc(Ows + (size_t)((br * NB + b) * NH + (h0 + hsel)) * 32768u, 32768u);
  const unsigned obo = (2u * qb + (l31 >> 4)) * 4096u + (l15 + 16u * hi) * 16u;
#pragma unroll
  for (int db = 0; db < 4; ++db)
#pragma unroll
    for (int u = 0; u < 2; ++u) {
      u32x4 w;
#pragma unroll
      for (int e = 0; e < 4; ++e) w[e] = cvtpk(o[db][8 * u + 2 * e] * inv, o[db][8 * u + 2 * e + 1] * inv);
      __builtin_amdgcn_raw_buffer_store_b128(w, rob, obo + db * 1024 + u * 512, 0, 16);
    }
  STAMP(28); CSTAMP(61); STAMP_FLUSH(Ows, 0u, 32u);
}

__global__ __launch_bounds__(512, 2) void k2_oproj(char* __restrict__ ws, const char* __restrict__ woA, const char* __restrict__ woB, char* __restrict__ out) {
  extern __shared__ __attribute__((aligned(16))) char lds[];
  const unsigned tid = threadIdx.x, lane = tid & 63u;
  const unsigned wid = __builtin_amdgcn_readfirstlane(tid >> 6);
  STAMP_DECL;
  STAMP(32); CSTAMP(62);
  const unsigned bi = blockIdx.x, xcd = bi & 7u, r = bi >> 3, br = xcd >> 2, hg = xcd & 3u, b = r & 7u, j = r >> 3;
  const unsigned L0 = (unsigned)(size_t)lds;
  const unsigned qb = wid & 3u, kh = wid >> 2;
  const rsrc_t rw = mkrsrc(br ? woB : woA, NH * DH * DM * 2);
  const rsrc_t ro = mkrsrc(ws + WS_O + (size_t)(br * NB + b) * NH * 32768u, NH * 32768u);
  unsigned vw[5];
#pragma unroll
  for (unsigned i = 0; i < 5; ++i) { const unsigned c = 64u * (qb + 4u * i) + lane; vw[i] = (c / 18u) * 1152u + (c % 18u) * 16u; }
#define ROT2J 2
  auto kpos = [&](int u) __attribute__((always_inline)) { return ((unsigned)u + b + ROT2J * j) & 7u; };
  const bool lowq = qb < 2u;
  const unsigned ldst = L0 + kh * 73728u + qb * 1024u;
  auto stage1 = [&](int u, int i) __attribute__((always_inline)) {
    if (i == 4 && !lowq) return;
    const unsigned so = ((hg * 8u + kh * 4u) * 128u + kpos(u) * 64u) * 1152u + j * 288u;
    dma16<W2_NT, 0>(ldst + (unsigned)(u & 3) * 18432u + (unsigned)i * 4096u, vw[i], rw, so);
  };
  const unsigned vo0 = lane * 16u + 2u * qb * 4096u, vo1 = vo0 + 4096u;
  bf16x8 bf[4][4];
  auto loadB = [&](int u, int f) __attribute__((always_inline)) {
    const unsigned kp = kpos(u), so = (hg * 8u + kh * 4u + (kp >> 1)) * 32768u + 2u * (kp & 1u) * 1024u;
    if (f == 0) asm volatile("buffer_load_dwordx4 %0, %1, %2, %3 offen" : "=v"(bf[u & 3][0]) : "v"(vo0), "s"(ro), "s"(so) : "memory");
    if (f == 1) asm volatile("buffer_load_dwordx4 %0, %1, %2, %3 offen" : "=v"(bf[u & 3][1]) : "v"(vo1), "s"(ro), "s"(so) : "memory");
    if (f == 2) asm volatile("buffer_load_dwordx4 %0, %1, %2, %3 offen offset:1024" : "=v"(bf[u & 3][2]) : "v"(vo0), "s"(ro), "s"(so) : "memory");
    if (f == 3) asm volatile("buffer_load_dwordx4 %0, %1, %2, %3 offen offset:1024" : "=v"(bf[u & 3][3]) : "v"(vo1), "s"(ro), "s"(so) : "memory");
  };
  auto issue = [&](int u, int v) __attribute__((always_inline)) {
    if (u > 7) return;
    if (v < 4) loadB(u, v); else if (v < 9) stage1(u, v - 4);
  };
  const unsigned aw = L0 + 288u * (16u * (lane >> 5) + 4u * ((lane >> 4) & 1u) + ((lane & 15u) >> 2)) + 8u * (lane & 3u) + kh * 73728u;
  f32x4 acc[9][2] = {};
  constexpr int DW = 6;
  bf16x8 fa[DW];
  auto rdW = [&](int F) __attribute__((always_inline)) {
    const int u = F / 18, e = (F % 18) / 9, nb = F % 9;
    const unsigned buf = aw + (unsigned)(u & 3) * 18432u + (unsigned)e * 9216u;
    fa[F % DW] = cat(lds_tr(buf + 32u * nb), lds_tr(buf + 288u * 8u + 32u * nb));
  };
  auto waitK2 = [&](int u, int N) __attribute__((always_inline)) {
    bf16x8* c = bf[u & 3];
    if (N == 0) WAITV(0);
    else { if (lowq) WAITV(9); else WAITV(8); }
    asm volatile("s_barrier" : "+v"(c[0]), "+v"(c[1]), "+v"(c[2]), "+v"(c[3]) :: "memory");
  };
  STAMP(51);
#pragma unroll
  for (int v = 0; v < 9; ++v) issue(0, v);
  STAMP(52);
  WAITV(0);
  asm volatile("s_barrier" : "+v"(bf[0][0]), "+v"(bf[0][1]), "+v"(bf[0][2]), "+v"(bf[0][3]) :: "memory");
  STAMP(33);
#pragma unroll
  for (int v = 0; v < 9; ++v) issue(1, v);
#pragma unroll
  for (int F = 0; F < DW; ++F) rdW(F);
  sfor<144>([&](auto Fc) __attribute__((always_inline)) {
    constexpr int F = decltype(Fc)::value, u = F / 18, p = F % 18, e = p / 9, nb = p % 9;
    if constexpr (p == 18 - DW && u < 7) { SB(); waitK2(u + 1, u + 2 <= 7 ? 1 : 0); STAMP(34 + u); }
    SB();
    acc[nb][0] = MFMA16(fa[F % DW], bf[u & 3][2 * e], acc[nb][0]);
    acc[nb][1] = MFMA16(fa[F % DW], bf[u & 3][2 * e + 1], acc[nb][1]);
    SB();
    if constexpr (F + DW < 144) rdW(F + DW);
    SB();
    if constexpr (p < 9) issue(u + 2, p);
  });
  SB();
  BARRIER();
  STAMP(41);
  LAS f32x4* cb = (LAS f32x4*)(size_t)(L0 + wid * 9216u + lane * 16u);
  LAS f32x4* cr = (LAS f32x4*)(size_t)(L0 + (wid ^ 4u) * 9216u + lane * 16u);
#pragma unroll
  for (int nb = 0; nb < 9; ++nb) cb[nb * 64] = kh ? acc[nb][0] : acc[nb][1];
  BARRIER();
  f32x4 tot[9];
#pragma unroll
  for (int nb = 0; nb < 9; ++nb) tot[nb] = kh ? cr[nb * 64] + acc[nb][1] : acc[nb][0] + cr[nb * 64];
  STAMP(45);
  const unsigned grp = (br * NB + b) * 4u + j;
  const rsrc_t rp = mkrsrc(ws + WS_P + (size_t)grp * 16u * 18432u, 16u * 18432u);
  GAS unsigned* cnt = (GAS unsigned*)(ws + WS_C + (size_t)grp * 4u * 128u);
  LAS f32x4* own = (LAS f32x4*)(size_t)(L0 + 73728u + lane * 16u);
  if (qb != hg) {
#pragma unroll
    for (int nb = 0; nb < 9; ++nb)
      __builtin_amdgcn_raw_buffer_store_b128(__builtin_bit_cast(u32x4, tot[nb]), rp, lane * 16u + (unsigned)nb * 1024u, (qb * 4u + hg) * 18432u + kh * 9216u, 16);
    STAMP(46);
    WAITV(0);
    STAMP(47);
    if (lane == 0) __hip_atomic_fetch_add(cnt + qb * 32u, 1u, __ATOMIC_RELAXED, __HIP_MEMORY_SCOPE_AGENT);
  } else {
#pragma unroll
    for (int nb = 0; nb < 9; ++nb) own[(kh * 9u + nb) * 64] = tot[nb];
    if (kh == 0) {
      unsigned spins = 0;
      while (__hip_atomic_load(cnt + hg * 32u, __ATOMIC_RELAXED, __HIP_MEMORY_SCOPE_AGENT) < 6u && ++spins < (1u << 20)) __builtin_amdgcn_s_sleep(1);
    }
  }
  STAMP(43);
  BARRIER();
  STAMP(44);
  {
    u32x4 pr[3][3];
    const bool third = wid < 2u;
#pragma unroll
    for (int i = 0; i < 3; ++i) {
      const unsigned tau = (i < 2 || third) ? wid + 8u * i : wid;
#pragma unroll
      for (unsigned s = 0; s < 3; ++s) { const unsigned src = s + (s >= hg ? 1u : 0u); pr[i][s] = __builtin_amdgcn_raw_buffer_load_b128(rp, lane * 16u + tau * 1024u, (hg * 4u + src) * 18432u, 16); }
    }
#pragma unroll
    for (int i = 0; i < 3; ++i) {
      if (i < 2 || third) {
        const unsigned tau = wid + 8u * i;
        const f32x4 v = own[tau * 64];
        const f32x4 p0 = __builtin_bit_cast(f32x4, pr[i][0]), p1 = __builtin_bit_cast(f32x4, pr[i][1]), p2 = __builtin_bit_cast(f32x4, pr[i][2]);
        const f32x4 t0 = hg == 0 ? v : p0, t1 = hg == 0 ? p0 : (hg == 1 ? v : p1), t2 = hg <= 1 ? p1 : (hg == 2 ? v : p2), t3 = hg == 3 ? v : p2;
        const f32x4 sum = (t0 + t1) + (t2 + t3);
        const unsigned qi = tau / 9u, nb = tau % 9u;
        const unsigned q = 32u * hg + 16u * qi + (lane & 15u), n = br * 576u + j * 144u + nb * 16u + 4u * (lane >> 4);
        u32x2 w = {cvtpk(sum[0], sum[1]), cvtpk(sum[2], sum[3])};
        *(GAS u32x2*)(out + ((size_t)(b * SQ + q) * 1152u + n) * 2u) = w;
      }
    }
  }
  STAMP(42); CSTAMP(63); STAMP_FLUSH((char*)ws, 32u, 48u);
}

extern "C" void kernel_launch(void* const* d_in, const int* in_sizes, int n_in, void* d_out, int out_size, void* d_ws, size_t ws_size, hipStream_t stream) {
  static bool attr = false;
  if (!attr) {
    (void)hipFuncSetAttribute((const void*)k1_qattn, hipFuncAttributeMaxDynamicSharedMemorySize, 163840);
    (void)hipFuncSetAttribute((const void*)k2_oproj, hipFuncAttributeMaxDynamicSharedMemorySize, 147456);
    attr = true;
  }
  char* ws = (char*)d_ws;
  k1_qattn<<<256, 512, 163840, stream>>>((const char*)d_in[0], (const char*)d_in[5], (const char*)d_in[1], (const char*)d_in[6], (const char*)d_in[2], (const char*)d_in[7],
                                         (const char*)d_in[3], (const char*)d_in[8], (const char*)d_in[4], (const char*)d_in[9], ws + WS_O);
  k2_oproj<<<256, 512, 147456, stream>>>(ws, (const char*)d_in[4], (const char*)d_in[9], (char*)d_out);
}
```

```cpp
#include <hip/hip_runtime.h>
#include <stdint.h>
#include <type_traits>
#include <utility>

#define LAS __attribute__((address_space(3)))
#define GAS __attribute__((address_space(1)))
#define DI __device__ __forceinline__
typedef __attribute__((ext_vector_type(8))) __bf16 bf16x8;
typedef __attribute__((ext_vector_type(4))) __bf16 bf16x4;
typedef __attribute__((ext_vector_type(2))) __bf16 bf16x2;
typedef __attribute__((ext_vector_type(16))) float f32x16;
typedef __attribute__((ext_vector_type(4))) float f32x4;
typedef __attribute__((ext_vector_type(2))) float f32x2;
typedef __attribute__((ext_vector_type(4))) unsigned u32x4;
typedef __attribute__((ext_vector_type(2))) unsigned u32x2;
typedef __amdgpu_buffer_rsrc_t rsrc_t;

constexpr int NB = 8, SQ = 128, DM = 576, NH = 32, SK = 512, DH = 128;
constexpr int SLOT = 16384;
constexpr size_t O_BYTES = (size_t)2 * NB * NH * SQ * DH * 2;
constexpr size_t WS_O = 0, WS_P = O_BYTES;
constexpr size_t P_BYTES = (size_t)64 * 4 * 4 * 18432;
constexpr size_t WS_FO = WS_P + P_BYTES, WS_FP = WS_FO + 1024 * 128;
constexpr float CSC = 0.08838834764831845f * 1.4426950408889634f;
constexpr float THR = 8.0f;

extern "C" __device__ unsigned long long hip_dispatch_id() __asm("llvm.amdgcn.dispatch.id");
DI unsigned cvtpk(float lo, float hi) { f32x2 v = {lo, hi}; bf16x2 b = __builtin_convertvector(v, bf16x2); return __builtin_bit_cast(unsigned, b); }
DI rsrc_t mkrsrc(const void* p, unsigned bytes) { return __builtin_amdgcn_make_buffer_rsrc((void*)p, (short)0, (int)bytes, 0x00020000); }
template <bool NT, int IMM> DI void dma16(unsigned ldsaddr, unsigned voff, rsrc_t r, unsigned soff) {
  if constexpr (NT) asm volatile("s_mov_b32 m0, %0\n\ts_nop 0\n\tbuffer_load_dwordx4 %1, %2, %3 offen offset:%c4 nt lds" ::"s"(ldsaddr), "v"(voff), "s"(r), "s"(soff), "i"(IMM) : "memory");
  else asm volatile("s_mov_b32 m0, %0\n\ts_nop 0\n\tbuffer_load_dwordx4 %1, %2, %3 offen offset:%c4 lds" ::"s"(ldsaddr), "v"(voff), "s"(r), "s"(soff), "i"(IMM) : "memory");
}
template <bool NT> DI void dma16i(int imm, unsigned ldsaddr, unsigned voff, rsrc_t r, unsigned soff) {
  switch (imm) {
    case 0: dma16<NT, 0>(ldsaddr, voff, r, soff); break;
    case 1: dma16<NT, 1024>(ldsaddr, voff, r, soff); break;
    case 2: dma16<NT, 2048>(ldsaddr, voff, r, soff); break;
    default: dma16<NT, 3072>(ldsaddr, voff, r, soff); break;
  }
}
template <int... I, class F> DI void sfor_impl(std::integer_sequence<int, I...>, F&& f) { (f(std::integral_constant<int, I>{}), ...); }
template <int N, class F> DI void sfor(F&& f) { sfor_impl(std::make_integer_sequence<int, N>{}, f); }
DI unsigned opq(unsigned x) { asm volatile("" : "+s"(x)); return x; }
#define WAITV(n) asm volatile("s_waitcnt vmcnt(" #n ")" ::: "memory")
#define BARRIER() asm volatile("s_waitcnt lgkmcnt(0)\n\ts_barrier" ::: "memory")
DI bf16x8 lds_b128(unsigned a) { return *(LAS bf16x8*)(size_t)a; }
DI bf16x4 lds_tr(unsigned a) { return __builtin_amdgcn_ds_read_tr16_b64_v4bf16((LAS bf16x4*)(size_t)a); }
DI bf16x8 cat(bf16x4 a, bf16x4 b) { return __builtin_shufflevector(a, b, 0, 1, 2, 3, 4, 5, 6, 7); }
DI float swapmax(float v) { auto r = __builtin_amdgcn_permlane32_swap(__float_as_uint(v), __float_as_uint(v), false, false); return fmaxf(__uint_as_float(r[0]), __uint_as_float(r[1])); }
DI float swapsum(float v) { auto r = __builtin_amdgcn_permlane32_swap(__float_as_uint(v), __float_as_uint(v), false, false); return __uint_as_float(r[0]) + __uint_as_float(r[1]); }
#define SB() __builtin_amdgcn_sched_barrier(0)
DI float max2f(float a, float b) { return __builtin_elementwise_maximum(a, b); }
DI float max3f(float a, float b, float c) { return max2f(max2f(a, b), c); }
#define MFMA32(a, b, c) __builtin_amdgcn_mfma_f32_32x32x16_bf16(a, b, c, 0, 0, 0)
#define MFMA16(a, b, c) __builtin_amdgcn_mfma_f32_16x16x32_bf16(a, b, c, 0, 0, 0)

#define STAMP(i) do {} while (0)
#define CSTAMP(i) do {} while (0)
#define STAMP_DECL do {} while (0)
#define ISTAMP(i) do {} while (0)
#define STAMP_FLUSH(base, lo, hi) do {} while (0)
#define KV_NT true
#define W_NT false
#define LOOKAHEAD 8
#define W2_NT false
#define KVSTART 1

__global__ __launch_bounds__(512, 2) void k_fused(const char* __restrict__ xA, const char* __restrict__ xB, const char* __restrict__ kA, const char* __restrict__ kB,
                                                   const char* __restrict__ vA, const char* __restrict__ vB, const char* __restrict__ wqA, const char* __restrict__ wqB,
                                                   const char* __restrict__ woA, const char* __restrict__ woB, char* __restrict__ Ows, char* __restrict__ out) {
  extern __shared__ __attribute__((aligned(16))) char lds[];
  const unsigned tid = threadIdx.x, lane = tid & 63u;
  const unsigned wid = __builtin_amdgcn_readfirstlane(tid >> 6);
  STAMP_DECL;
  STAMP(0); CSTAMP(60);
  const unsigned bi = blockIdx.x, xcd = bi & 7u, r = bi >> 3, br = xcd >> 2, hg = xcd & 3u, b = r & 7u, hp = r >> 3, h0 = hg * 8u + hp * 2u;
  const unsigned L0 = (unsigned)(size_t)lds;
  const rsrc_t rx = mkrsrc((br ? xB : xA) + (size_t)b * SQ * DM * 2, SQ * DM * 2);
  const rsrc_t rw = mkrsrc(br ? wqB : wqA, DM * NH * DH * 2);
  const rsrc_t rk = mkrsrc((br ? kB : kA) + (size_t)(b * NH + h0) * SK * DH * 2, 2 * SK * DH * 2);
  const rsrc_t rv = mkrsrc((br ? vB : vA) + (size_t)(b * NH + h0) * SK * DH * 2, 2 * SK * DH * 2);
  const unsigned l15 = lane & 15u, l31 = lane & 31u, hi = lane >> 5, g4 = lane >> 4, blk = g4 & 1u, qq = l15 >> 2, pp = lane & 3u;
  const unsigned hsel = wid >> 2, qb = wid & 3u;

  unsigned vx[2], vw[2];
#pragma unroll
  for (unsigned j = 0; j < 2; ++j) {
    vx[j] = (lane >> 3) * 1152u + (((lane & 7u) ^ (4u * j + g4)) << 4) + j * (9216u - 1024u);
    vw[j] = g4 * 8192u + ((l15 ^ (g4 << 2)) << 4) + j * (32768u - 1024u);
  }
  const unsigned vv = g4 * 256u + ((l15 ^ (g4 << 2)) << 4);
  auto vk = [&](unsigned p) __attribute__((always_inline)) { return g4 * 256u + ((l15 ^ ((4u * (p & 3u) + g4) & 15u)) << 4); };
  const unsigned vkA = vk(2u * wid), vkB = vk(2u * wid + 1u);
  unsigned w2k = wid * 2048u;
  auto kv_piece = [&](bool isV, unsigned head, unsigned t, unsigned slot, int j) __attribute__((always_inline)) {
    const unsigned so = head * 131072u + t * 16384u + w2k, la = L0 + slot * SLOT + w2k;
    if (isV) dma16i<KV_NT>(j, la, vv, rv, so);
    else dma16i<KV_NT>(j, la, j ? vkB : vkA, rk, so);
  };

  const unsigned rot = b + 2u * hp + 7u;
  auto stage1 = [&](int i, int j) __attribute__((always_inline)) {
    const unsigned ks = i == 0 ? 0u : 1u + ((rot + (unsigned)i) & 7u);
    const unsigned buf = L0 + (unsigned)((i % 3) * 3) * SLOT + w2k;
    if (j < 2) dma16i<W_NT>(j, buf, vx[j], rx, wid * 18432u + ks * 128u);
    else { const unsigned h = (j >> 1) - 1u; dma16i<W_NT>(j & 1, buf + (1u + h) * SLOT, vw[j & 1], rw, (ks * 64u + 8u * wid) * 8192u + (h0 + h) * 256u); }
  };
  auto issueA = [&](int i, int j) __attribute__((always_inline)) {
    if (i == -1) { if (j >= 4 && j < 8) stage1(2, j - 4); return; }
    if (i <= 5) { if (j < 6) stage1(i + 3, j); }
  };
  STAMP(48);
#pragma unroll
  for (int j = 0; j < 6; ++j) stage1(0, j);
  STAMP(49);
  asm volatile("s_barrier" ::: "memory");
#pragma unroll
  for (int j = 0; j < 6; ++j) stage1(1, j);
  STAMP(50);
  f32x16 acc[4] = {};
  const unsigned xrow = 32u * qb + l31;
  unsigned ax[4], aw[4];
#pragma unroll
  for (unsigned ks = 0; ks < 4; ++ks) ax[ks] = L0 + xrow * 128u + (((2u * ks + hi) ^ ((xrow >> 1) & 7u)) << 4);
#pragma unroll
  for (unsigned db = 0; db < 4; ++db) aw[db] = L0 + 256u * (8u * hi + qq) + (((4u * db + 2u * blk + (pp & 1u)) ^ (qq << 2)) << 4) + 8u * (pp >> 1) + (1u + hsel) * SLOT;
  {
    bf16x8 fx[2], fa[2][4];
    auto rdA = [&](int i, int ks, int db) __attribute__((always_inline)) {
      const unsigned buf = (unsigned)((i % 3) * 3) * SLOT;
      if (db == 0) fx[ks & 1] = lds_b128(buf + ax[ks]);
      fa[ks & 1][db] = cat(lds_tr(buf + aw[db] + 256u * (16u * ks)), lds_tr(buf + aw[db] + 256u * (16u * ks + 4u)));
    };
    WAITV(6);
    BARRIER();
    STAMP(1); CSTAMP(54);
#pragma unroll
    for (int db = 0; db < 4; ++db) rdA(0, 0, db);
#pragma unroll
    for (int i = 0; i < 9; ++i) {
#pragma unroll
      for (int ks = 0; ks < 4; ++ks) {
        if (ks == 3 && i < 8) {
          WAITV(6);
          BARRIER();
          STAMP(2 + i); if (i == 7) CSTAMP(55);
        }
#pragma unroll
        for (int db = 0; db < 4; ++db) {
          SB();
          acc[db] = MFMA32(fa[ks & 1][db], fx[ks & 1], acc[db]);
          SB();
          if (ks < 3) rdA(i, ks + 1, db); else if (i < 8) rdA(i + 1, 0, db);
          SB();
          if (ks == 3) issueA(i, db); else if (ks == 0) issueA(i - 1, 4 + db);
          if (i == 0 && ks == 1 && db < 2) stage1(2, 4 + db);
          if (i == 6 && ks == 3) { if (db < 2) kv_piece(false, 0, 0, 9, db); else kv_piece(false, 1, 0, 1, db - 2); }
          if (i == 7 && ks == 0 && db < 2) kv_piece(true, 0, 0, 2, db);
          if (i == 7 && ks == 3) { if (db < 2) kv_piece(false, 0, 1, 3, db); else kv_piece(true, 1, 0, 4, db - 2); }
        }
        SB();
      }
    }
  }
  bf16x8 qf[8];
#pragma unroll
  for (int s = 0; s < 8; ++s) {
    u32x4 w;
#pragma unroll
    for (int e = 0; e < 4; ++e) w[e] = cvtpk(acc[s >> 1][8 * (s & 1) + 2 * e] * CSC, acc[s >> 1][8 * (s & 1) + 2 * e + 1] * CSC);
    qf[s] = __builtin_bit_cast(bf16x8, w);
  }

  STAMP(10); CSTAMP(56);
  w2k = opq(wid * 2048u);
  unsigned ak[8], av[4];
#pragma unroll
  for (unsigned s = 0; s < 8; ++s) ak[s] = 256u * l31 + (((2u * s + hi) ^ l15) << 4);
#pragma unroll
  for (unsigned db = 0; db < 4; ++db) av[db] = 256u * (4u * hi + qq) + (((4u * db + 2u * blk + (pp >> 1)) ^ (qq << 2)) << 4) + 8u * (pp & 1u);
  f32x16 o[4] = {};
  float m = 0.f, l = 0.f;
  f32x16 S[2], negm = {};
  bf16x8 pf[4];
  unsigned sn = 5u;
  auto nxt = [](unsigned s_) __attribute__((always_inline)) { return s_ == 9u ? 0u : s_ + 1u; };
  unsigned s0 = 0, s1 = 0, s2 = 0, s3 = 0;
  auto slots = [&]() __attribute__((always_inline)) { s0 = sn; s1 = nxt(s0); s2 = nxt(s1); s3 = nxt(s2); sn = nxt(s3); };
  const rsrc_t rwo = mkrsrc((br ? woB : woA) + (size_t)hg * 1024u * 1152u, 1024u * 1152u);
  auto warmWo = [&]() __attribute__((always_inline)) {
#pragma unroll
    for (int i = 0; i < 4; ++i) dma16i<false>(i, L0 + s0 * SLOT + w2k, lane * 16u, rwo, r * 36864u + wid * 5120u);
    dma16<false, 0>(L0 + s1 * SLOT + w2k, lane * 16u, rwo, r * 36864u + wid * 5120u + 4096u);
  };
  auto issueB = [&](int t, int j) __attribute__((always_inline)) {
    const int q = j >> 1;
    if (j == 0 && t == 7) warmWo();
    if (q == 0) { if (t < 7) kv_piece(false, 1, t + 1, s0, j & 1); }
    else if (q == 1) { if (t < 7) kv_piece(true, 0, t + 1, s1, j & 1); }
    else if (q == 2) { if (t < 6) kv_piece(false, 0, t + 2, s2, j & 1); }
    else { if (t < 7) kv_piece(true, 1, t + 1, s3, j & 1); }
  };
  unsigned curK = 0, curV = 0;
  auto setK = [&](unsigned kb0) __attribute__((always_inline)) {
    const unsigned d = kb0 - curK; curK = kb0;
#pragma unroll
    for (int i = 0; i < 8; ++i) { ak[i] += d; asm volatile("" : "+v"(ak[i])); }
  };
  auto setV = [&](unsigned vb0) __attribute__((always_inline)) {
    const unsigned d = vb0 - curV; curV = vb0;
#pragma unroll
    for (int i = 0; i < 4; ++i) { av[i] += d; asm volatile("" : "+v"(av[i])); }
  };
  constexpr int DL = LOOKAHEAD;
  bf16x8 kf[DL], vf[DL];
  auto rdK = [&](int f) __attribute__((always_inline)) { kf[f % DL] = lds_b128(ak[f & 7] + 8192u * (f >> 3)); };
  auto rdV = [&](int f) __attribute__((always_inline)) { vf[f % DL] = cat(lds_tr(av[f & 3] + 256u * (16u * (f >> 2))), lds_tr(av[f & 3] + 256u * (16u * (f >> 2) + 8u))); };
  auto QK = [&](int t, int j0, bool nextV) __attribute__((always_inline)) {
#pragma unroll
    for (int f = 0; f < 16; ++f) {
      SB();
      if ((f & 7) == 0) S[f >> 3] = MFMA32(kf[f % DL], qf[f & 7], negm);
      else S[f >> 3] = MFMA32(kf[f % DL], qf[f & 7], S[f >> 3]);
      SB();
      if (f + DL < 16) rdK(f + DL); else if (nextV) rdV(f + DL - 16);
      SB();
      if ((f & 3) == 1) issueB(t, j0 + (f >> 2));
    }
    SB();
  };
  auto SM = [&](bool first) __attribute__((always_inline)) {
    SB(); __builtin_amdgcn_s_setprio(2); SB();
    float pm0 = max3f(S[0][0], S[0][1], S[1][0]), pm1 = max3f(S[0][2], S[0][3], S[1][1]);
    pm0 = max3f(pm0, S[1][2], S[1][3]);
#pragma unroll
    for (int e = 4; e < 16; e += 4) {
      pm0 = max3f(pm0, S[0][e], S[0][e + 1]); pm1 = max3f(pm1, S[0][e + 2], S[0][e + 3]);
      pm0 = max3f(pm0, S[1][e], S[1][e + 1]); pm1 = max3f(pm1, S[1][e + 2], S[1][e + 3]);
    }
    float pm = max2f(pm0, pm1);
    { auto rr = __builtin_amdgcn_permlane32_swap(__float_as_uint(pm), __float_as_uint(pm), false, false); pm = max2f(__uint_as_float(rr[0]), __uint_as_float(rr[1])); }
    if (first || __builtin_expect(__any(!(pm <= THR)), 0)) {
      const float dl = first ? pm : max2f(pm, 0.f);
      m += dl;
      if (!first) {
        const float al = __builtin_amdgcn_exp2f(-dl);
        l *= al;
#pragma unroll
        for (int db = 0; db < 4; ++db)
#pragma unroll
          for (int e = 0; e < 16; ++e) o[db][e] *= al;
      }
#pragma unroll
      for (int e = 0; e < 16; ++e) { S[0][e] -= dl; S[1][e] -= dl; negm[e] = -m; }
    }
    float l0 = 0.f, l1 = 0.f;
#pragma unroll
    for (int kb = 0; kb < 2; ++kb) {
#pragma unroll
      for (int e = 0; e < 16; e += 2) {
        S[kb][e] = __builtin_amdgcn_exp2f(S[kb][e]); l0 += S[kb][e];
        S[kb][e + 1] = __builtin_amdgcn_exp2f(S[kb][e + 1]); l1 += S[kb][e + 1];
      }
#pragma unroll
      for (int u = 0; u < 2; ++u) {
        u32x4 w;
#pragma unroll
        for (int e = 0; e < 4; ++e) w[e] = cvtpk(S[kb][8 * u + 2 * e], S[kb][8 * u + 2 * e + 1]);
        pf[2 * kb + u] = __builtin_bit_cast(bf16x8, w);
      }
    }
    l += l0 + l1;
    asm volatile("" : "+v"(pf[0]), "+v"(pf[1]), "+v"(pf[2]), "+v"(pf[3]), "+v"(l));
    SB(); __builtin_amdgcn_s_setprio(0); SB();
  };
  auto waitB = [&](int t) __attribute__((always_inline)) { if (t == 8) WAITV(0); else if (t == 7) WAITV(2); else WAITV(4); BARRIER(); };
  if (hsel == 0) {
    unsigned sK = 9u, sV = 2u;
    auto stepA = [&](int t, auto firstc) __attribute__((always_inline)) {
      constexpr bool FIRST = decltype(firstc)::value;
      waitB(t);
      STAMP(11 + t);
      ISTAMP(47);
      slots();
      setK(L0 + sK * SLOT);
#pragma unroll
      for (int i = 0; i < 4; ++i) {
        SB();
#pragma unroll
        for (int f = i * DL / 4; f < (i + 1) * DL / 4; ++f) rdK(f);
        SB();
        if (!FIRST) o[i] = MFMA32(vf[(12 + i) % DL], pf[3], o[i]);
      }
      SB();
      setV(L0 + sV * SLOT);
      ISTAMP(43);
      QK(t, 0, true);
      ISTAMP(44);
      SM(FIRST);
      ISTAMP(45);
#pragma unroll
      for (int f = 0; f < 12; ++f) {
        SB();
        o[f & 3] = MFMA32(vf[f % DL], pf[f >> 2], o[f & 3]);
        SB();
        if (f + DL < 16) rdV(f + DL);
        SB();
        if ((f & 3) == 1) issueB(t, 4 + (f >> 2));
        if (f == 11) issueB(t, 7);
      }
      SB();
      ISTAMP(46);
      sK += 4u; if (sK >= 10u) sK -= 10u;
      sV += 4u; if (sV >= 10u) sV -= 10u;
    };
    stepA(0, std::true_type{});
#pragma unroll 1
    for (int t = 1; t < 8; ++t) stepA(t, std::false_type{});
    waitB(8);
    STAMP(19);
#pragma unroll
    for (int i = 0; i < 4; ++i) o[i] = MFMA32(vf[(12 + i) % DL], pf[3], o[i]);
  } else {
    unsigned sK = 1u, sV = 0u;
    auto stepB = [&](int t, auto firstc, auto lastc) __attribute__((always_inline)) {
      constexpr bool FIRST = decltype(firstc)::value, LAST = decltype(lastc)::value;
      waitB(t);
      STAMP(11 + t);
      if (!LAST) ISTAMP(47);
      slots();
      setV(L0 + sV * SLOT);
#pragma unroll
      for (int f = 0; f < DL; ++f) rdV(f);
      if (!LAST) setK(L0 + sK * SLOT);
      SB();
      if (!LAST) ISTAMP(43);
      SM(FIRST);
      if (!LAST) ISTAMP(44);
#pragma unroll
      for (int f = 0; f < 16; ++f) {
        SB();
        o[f & 3] = MFMA32(vf[f % DL], pf[f >> 2], o[f & 3]);
        SB();
        if (f + DL < 16) rdV(f + DL); else if (!LAST) rdK(f + DL - 16);
        SB();
        if ((f & 3) == 1 && !LAST) issueB(t, f >> 2);
      }
      if (!LAST) ISTAMP(45);
      if (!LAST) QK(t, 4, false);
      SB();
      if (!LAST) ISTAMP(46);
      sK += 4u; if (sK >= 10u) sK -= 10u;
      sV += 4u; if (sV >= 10u) sV -= 10u;
    };
    {
      waitB(0);
      STAMP(11);
      slots();
      setK(L0 + sK * SLOT);
#pragma unroll
      for (int f = 0; f < DL; ++f) rdK(f);
#pragma unroll
      for (int j = 0; j < 4; ++j) issueB(0, j);
      QK(0, 4, false);
      sK = 5u; sV = 4u;
    }
    stepB(1, std::true_type{}, std::false_type{});
#pragma unroll 1
    for (int t = 2; t < 8; ++t) stepB(t, std::false_type{}, std::false_type{});
    stepB(8, std::false_type{}, std::true_type{});
  }
  STAMP(27); CSTAMP(57);
  l = swapsum(l);
  const float inv = __builtin_amdgcn_rcpf(l);
  const rsrc_t rob = mkrsrc(Ows + (size_t)((br * NB + b) * NH + (h0 + hsel)) * 32768u, 32768u);
  const unsigned obo = (2u * qb + (l31 >> 4)) * 4096u + (l15 + 16u * hi) * 16u;
#pragma unroll
  for (int db = 0; db < 4; ++db)
#pragma unroll
    for (int u = 0; u < 2; ++u) {
      u32x4 w;
#pragma unroll
      for (int e = 0; e < 4; ++e) w[e] = cvtpk(o[db][8 * u + 2 * e] * inv, o[db][8 * u + 2 * e + 1] * inv);
      __builtin_amdgcn_raw_buffer_store_b128(w, rob, obo + db * 1024 + u * 512, 0, 16);
    }
  STAMP(28); CSTAMP(61);

  {
  char* ws2 = Ows;
  const unsigned j = hp, kh = hsel;
  STAMP(32); CSTAMP(62);
  const rsrc_t rw = mkrsrc(br ? woB : woA, NH * DH * DM * 2);
  const rsrc_t ro = mkrsrc(ws2 + WS_O + (size_t)(br * NB + b) * NH * 32768u, NH * 32768u);
  unsigned vw[5];
#pragma unroll
  for (unsigned i = 0; i < 5; ++i) { const unsigned c = 64u * (qb + 4u * i) + lane; vw[i] = (c / 18u) * 1152u + (c % 18u) * 16u; }
#define ROT2J 2
  auto kpos = [&](int u) __attribute__((always_inline)) { return ((unsigned)u + b + ROT2J * j) & 7u; };
  const bool lowq = qb < 2u;
  const unsigned ldst = L0 + kh * 73728u + qb * 1024u;
  auto stage1 = [&](int u, int i) __attribute__((always_inline)) {
    if (i == 4 && !lowq) return;
    const unsigned so = ((hg * 8u + kh * 4u) * 128u + kpos(u) * 64u) * 1152u + j * 288u;
    dma16<W2_NT, 0>(ldst + (unsigned)(u & 3) * 18432u + (unsigned)i * 4096u, vw[i], rw, so);
  };
  const unsigned vo0 = lane * 16u + 2u * qb * 4096u, vo1 = vo0 + 4096u;
  bf16x8 bf[4][4];
  auto loadB = [&](int u, int f) __attribute__((always_inline)) {
    const unsigned kp = kpos(u), so = (hg * 8u + kh * 4u + (kp >> 1)) * 32768u + 2u * (kp & 1u) * 1024u;
    if (f == 0) asm volatile("buffer_load_dwordx4 %0, %1, %2, %3 offen sc1" : "=v"(bf[u & 3][0]) : "v"(vo0), "s"(ro), "s"(so) : "memory");
    if (f == 1) asm volatile("buffer_load_dwordx4 %0, %1, %2, %3 offen sc1" : "=v"(bf[u & 3][1]) : "v"(vo1), "s"(ro), "s"(so) : "memory");
    if (f == 2) asm volatile("buffer_load_dwordx4 %0, %1, %2, %3 offen offset:1024 sc1" : "=v"(bf[u & 3][2]) : "v"(vo0), "s"(ro), "s"(so) : "memory");
    if (f == 3) asm volatile("buffer_load_dwordx4 %0, %1, %2, %3 offen offset:1024 sc1" : "=v"(bf[u & 3][3]) : "v"(vo1), "s"(ro), "s"(so) : "memory");
  };
  auto issue = [&](int u, int v) __attribute__((always_inline)) {
    if (u > 7) return;
    if (v < 4) loadB(u, v); else if (v < 9) stage1(u, v - 4);
  };
  const unsigned aw = L0 + 288u * (16u * (lane >> 5) + 4u * ((lane >> 4) & 1u) + ((lane & 15u) >> 2)) + 8u * (lane & 3u) + kh * 73728u;
  f32x4 acc[9][2] = {};
  constexpr int DW = 6;
  bf16x8 fa[DW];
  auto rdW = [&](int F) __attribute__((always_inline)) {
    const int u = F / 18, e = (F % 18) / 9, nb = F % 9;
    const unsigned buf = aw + (unsigned)(u & 3) * 18432u + (unsigned)e * 9216u;
    fa[F % DW] = cat(lds_tr(buf + 32u * nb), lds_tr(buf + 288u * 8u + 32u * nb));
  };
  auto waitK2 = [&](int u, int N) __attribute__((always_inline)) {
    bf16x8* c = bf[u & 3];
    if (N == 0) WAITV(0);
    else { if (lowq) WAITV(9); else WAITV(8); }
    asm volatile("s_barrier" : "+v"(c[0]), "+v"(c[1]), "+v"(c[2]), "+v"(c[3]) :: "memory");
  };
  const unsigned long long tag = ((unsigned long long)hip_dispatch_id() + 1ull) * 0x9E3779B97F4A7C15ull ^ 0x6A09E667F3BCC908ull;
  GAS unsigned long long* fo = (GAS unsigned long long*)(ws2 + WS_FO + (size_t)((br * NB + b) * 4u + hg) * 16u * 128u);
#pragma unroll
  for (int i = 0; i < 5; ++i) stage1(0, i);
  if (lowq) WAITV(5); else WAITV(4);
  BARRIER();
  if (wid == 0u && lane < 4u && lane != j) __hip_atomic_store(fo + (lane * 4u + j) * 16u, tag, __ATOMIC_RELAXED, __HIP_MEMORY_SCOPE_AGENT);
  STAMP(51);
#pragma unroll
  for (int i = 0; i < 5; ++i) stage1(1, i);
  if (wid == 0u) {
    const unsigned pl = lane & 3u;
    unsigned spins = 0;
    for (;;) {
      const unsigned long long v = pl == j ? tag : __hip_atomic_load(fo + (j * 4u + pl) * 16u, __ATOMIC_RELAXED, __HIP_MEMORY_SCOPE_AGENT);
      if (__all(v == tag) || ++spins > (1u << 18)) break;
      __builtin_amdgcn_s_sleep(1);
    }
    if (lane < 4u && lane != j) __hip_atomic_store(fo + (j * 4u + lane) * 16u, 0ull, __ATOMIC_RELAXED, __HIP_MEMORY_SCOPE_AGENT);
  }
  STAMP(52);
  BARRIER();
#pragma unroll
  for (int f = 0; f < 4; ++f) loadB(0, f);
#pragma unroll
  for (int f = 0; f < 4; ++f) loadB(1, f);
  WAITV(4);
  asm volatile("s_barrier" : "+v"(bf[0][0]), "+v"(bf[0][1]), "+v"(bf[0][2]), "+v"(bf[0][3]) :: "memory");
  STAMP(33);
#pragma unroll
  for (int F = 0; F < DW; ++F) rdW(F);
  sfor<144>([&](auto Fc) __attribute__((always_inline)) {
    constexpr int F = decltype(Fc)::value, u = F / 18, p = F % 18, e = p / 9, nb = p % 9;
    if constexpr (p == 18 - DW && u < 7) { SB(); waitK2(u + 1, u + 2 <= 7 ? 1 : 0); STAMP(34 + u); }
    SB();
    acc[nb][0] = MFMA16(fa[F % DW], bf[u & 3][2 * e], acc[nb][0]);
    acc[nb][1] = MFMA16(fa[F % DW], bf[u & 3][2 * e + 1], acc[nb][1]);
    SB();
    if constexpr (F + DW < 144) rdW(F + DW);
    SB();
    if constexpr (p < 9) issue(u + 2, p);
  });
  SB();
  BARRIER();
  STAMP(41);
  LAS f32x4* cb = (LAS f32x4*)(size_t)(L0 + wid * 9216u + lane * 16u);
  LAS f32x4* cr = (LAS f32x4*)(size_t)(L0 + (wid ^ 4u) * 9216u + lane * 16u);
#pragma unroll
  for (int nb = 0; nb < 9; ++nb) cb[nb * 64] = kh ? acc[nb][0] : acc[nb][1];
  BARRIER();
  f32x4 tot[9];
#pragma unroll
  for (int nb = 0; nb < 9; ++nb) tot[nb] = kh ? cr[nb * 64] + acc[nb][1] : acc[nb][0] + cr[nb * 64];
  STAMP(45);
  const unsigned grp = (br * NB + b) * 4u + j;
  const rsrc_t rp = mkrsrc(ws2 + WS_P + (size_t)grp * 16u * 18432u, 16u * 18432u);
  GAS unsigned long long* fp = (GAS unsigned long long*)(ws2 + WS_FP + (size_t)grp * 16u * 128u);
  LAS f32x4* own = (LAS f32x4*)(size_t)(L0 + 73728u + lane * 16u);
  if (qb != hg) {
#pragma unroll
    for (int nb = 0; nb < 9; ++nb)
      __builtin_amdgcn_raw_buffer_store_b128(__builtin_bit_cast(u32x4, tot[nb]), rp, lane * 16u + (unsigned)nb * 1024u, (qb * 4u + hg) * 18432u + kh * 9216u, 16);
    STAMP(46);
    WAITV(0);
    STAMP(47);
  } else {
#pragma unroll
    for (int nb = 0; nb < 9; ++nb) own[(kh * 9u + nb) * 64] = tot[nb];
  }
  BARRIER();
  if (wid == 0u) {
    if (lane < 4u && lane != hg) __hip_atomic_store(fp + (lane * 4u + hg) * 16u, tag, __ATOMIC_RELAXED, __HIP_MEMORY_SCOPE_AGENT);
    const unsigned pl = lane & 3u;
    unsigned spins = 0;
    for (;;) {
      const unsigned long long v = pl == hg ? tag : __hip_atomic_load(fp + (hg * 4u + pl) * 16u, __ATOMIC_RELAXED, __HIP_MEMORY_SCOPE_AGENT);
      if (__all(v == tag) || ++spins > (1u << 18)) break;
      __builtin_amdgcn_s_sleep(1);
    }
    if (lane < 4u && lane != hg) __hip_atomic_store(fp + (hg * 4u + lane) * 16u, 0ull, __ATOMIC_RELAXED, __HIP_MEMORY_SCOPE_AGENT);
  }
  STAMP(43);
  BARRIER();
  STAMP(44);
  {
    u32x4 pr[3][3];
    const bool third = wid < 2u;
#pragma unroll
    for (int i = 0; i < 3; ++i) {
      const unsigned tau = (i < 2 || third) ? wid + 8u * i : wid;
#pragma unroll
      for (unsigned s = 0; s < 3; ++s) { const unsigned src = s + (s >= hg ? 1u : 0u); pr[i][s] = __builtin_amdgcn_raw_buffer_load_b128(rp, lane * 16u + tau * 1024u, (hg * 4u + src) * 18432u, 16); }
    }
#pragma unroll
    for (int i = 0; i < 3; ++i) {
      if (i < 2 || third) {
        const unsigned tau = wid + 8u * i;
        const f32x4 v = own[tau * 64];
        const f32x4 p0 = __builtin_bit_cast(f32x4, pr[i][0]), p1 = __builtin_bit_cast(f32x4, pr[i][1]), p2 = __builtin_bit_cast(f32x4, pr[i][2]);
        const f32x4 t0 = hg == 0 ? v : p0, t1 = hg == 0 ? p0 : (hg == 1 ? v : p1), t2 = hg <= 1 ? p1 : (hg == 2 ? v : p2), t3 = hg == 3 ? v : p2;
        const f32x4 sum = (t0 + t1) + (t2 + t3);
        const unsigned qi = tau / 9u, nb = tau % 9u;
        const unsigned q = 32u * hg + 16u * qi + (lane & 15u), n = br * 576u + j * 144u + nb * 16u + 4u * (lane >> 4);
        u32x2 w = {cvtpk(sum[0], sum[1]), cvtpk(sum[2], sum[3])};
        *(GAS u32x2*)(out + ((size_t)(b * SQ + q) * 1152u + n) * 2u) = w;
      }
    }
  }
  STAMP(42); CSTAMP(63);
  }
}

extern "C" void kernel_launch(void* const* d_in, const int* in_sizes, int n_in, void* d_out, int out_size, void* d_ws, size_t ws_size, hipStream_t stream) {
  static bool attr = false;
  if (!attr) {
    (void)hipFuncSetAttribute((const void*)k_fused, hipFuncAttributeMaxDynamicSharedMemorySize, 163840);
    attr = true;
  }
  k_fused<<<256, 512, 163840, stream>>>((const char*)d_in[0], (const char*)d_in[5], (const char*)d_in[1], (const char*)d_in[6], (const char*)d_in[2], (const char*)d_in[7],
                                        (const char*)d_in[3], (const char*)d_in[8], (const char*)d_in[4], (const char*)d_in[9], (char*)d_ws + WS_O, (char*)d_out);
}
```

```cpp
#include <hip/hip_runtime.h>
#include <stdint.h>
#include <type_traits>
#include <utility>

#define LAS __attribute__((address_space(3)))
#define GAS __attribute__((address_space(1)))
#define DI __device__ __forceinline__
typedef __attribute__((ext_vector_type(8))) __bf16 bf16x8;
typedef __attribute__((ext_vector_type(4))) __bf16 bf16x4;
typedef __attribute__((ext_vector_type(2))) __bf16 bf16x2;
typedef __attribute__((ext_vector_type(16))) float f32x16;
typedef __attribute__((ext_vector_type(4))) float f32x4;
typedef __attribute__((ext_vector_type(2))) float f32x2;
typedef __attribute__((ext_vector_type(4))) unsigned u32x4;
typedef __attribute__((ext_vector_type(2))) unsigned u32x2;
typedef __amdgpu_buffer_rsrc_t rsrc_t;

constexpr int NB = 8, SQ = 128, DM = 576, NH = 32, SK = 512, DH = 128;
constexpr int SLOT = 16384;
constexpr size_t O_BYTES = (size_t)2 * NB * NH * SQ * DH * 2;
constexpr size_t WS_O = 0, WS_P = O_BYTES;
constexpr size_t P_BYTES = (size_t)64 * 4 * 4 * 18432;
constexpr size_t WS_FO = WS_P + P_BYTES, WS_FP = WS_FO + 1024 * 128;
constexpr float CSC = 0.08838834764831845f * 1.4426950408889634f;
constexpr float THR = 8.0f;

extern "C" __device__ unsigned long long hip_dispatch_id() __asm("llvm.amdgcn.dispatch.id");
DI unsigned cvtpk(float lo, float hi) { f32x2 v = {lo, hi}; bf16x2 b = __builtin_convertvector(v, bf16x2); return __builtin_bit_cast(unsigned, b); }
DI rsrc_t mkrsrc(const void* p, unsigned bytes) { return __builtin_amdgcn_make_buffer_rsrc((void*)p, (short)0, (int)bytes, 0x00020000); }
template <bool NT, int IMM> DI void dma16(unsigned ldsaddr, unsigned voff, rsrc_t r, unsigned soff) {
  if constexpr (NT) asm volatile("s_mov_b32 m0, %0\n\ts_nop 0\n\tbuffer_load_dwordx4 %1, %2, %3 offen offset:%c4 nt lds" ::"s"(ldsaddr), "v"(voff), "s"(r), "s"(soff), "i"(IMM) : "memory");
  else asm volatile("s_mov_b32 m0, %0\n\ts_nop 0\n\tbuffer_load_dwordx4 %1, %2, %3 offen offset:%c4 lds" ::"s"(ldsaddr), "v"(voff), "s"(r), "s"(soff), "i"(IMM) : "memory");
}
template <bool NT> DI void dma16i(int imm, unsigned ldsaddr, unsigned voff, rsrc_t r, unsigned soff) {
  switch (imm) {
    case 0: dma16<NT, 0>(ldsaddr, voff, r, soff); break;
    case 1: dma16<NT, 1024>(ldsaddr, voff, r, soff); break;
    case 2: dma16<NT, 2048>(ldsaddr, voff, r, soff); break;
    default: dma16<NT, 3072>(ldsaddr, voff, r, soff); break;
  }
}
template <int... I, class F> DI void sfor_impl(std::integer_sequence<int, I...>, F&& f) { (f(std::integral_constant<int, I>{}), ...); }
template <int N, class F> DI void sfor(F&& f) { sfor_impl(std::make_integer_sequence<int, N>{}, f); }
DI unsigned opq(unsigned x) { asm volatile("" : "+s"(x)); return x; }
#define WAITV(n) asm volatile("s_waitcnt vmcnt(" #n ")" ::: "memory")
#define BARRIER() asm volatile("s_waitcnt lgkmcnt(0)\n\ts_barrier" ::: "memory")
DI bf16x8 lds_b128(unsigned a) { return *(LAS bf16x8*)(size_t)a; }
DI bf16x4 lds_tr(unsigned a) { return __builtin_amdgcn_ds_read_tr16_b64_v4bf16((LAS bf16x4*)(size_t)a); }
DI bf16x8 cat(bf16x4 a, bf16x4 b) { return __builtin_shufflevector(a, b, 0, 1, 2, 3, 4, 5, 6, 7); }
DI float swapmax(float v) { auto r = __builtin_amdgcn_permlane32_swap(__float_as_uint(v), __float_as_uint(v), false, false); return fmaxf(__uint_as_float(r[0]), __uint_as_float(r[1])); }
DI float swapsum(float v) { auto r = __builtin_amdgcn_permlane32_swap(__float_as_uint(v), __float_as_uint(v), false, false); return __uint_as_float(r[0]) + __uint_as_float(r[1]); }
#define SB() __builtin_amdgcn_sched_barrier(0)
DI float max2f(float a, float b) { return __builtin_elementwise_maximum(a, b); }
DI float max3f(float a, float b, float c) { return max2f(max2f(a, b), c); }
#define MFMA32(a, b, c) __builtin_amdgcn_mfma_f32_32x32x16_bf16(a, b, c, 0, 0, 0)
#define MFMA16(a, b, c) __builtin_amdgcn_mfma_f32_16x16x32_bf16(a, b, c, 0, 0, 0)

#define STAMP(i) do {} while (0)
#define CSTAMP(i) do {} while (0)
#define STAMP_DECL do {} while (0)
#define ISTAMP(i) do {} while (0)
#define STAMP_FLUSH(base, lo, hi) do {} while (0)
#define KV_NT true
#define W_NT false
#define LOOKAHEAD 8
#define W2_NT false
#define KVSTART 1

__global__ __launch_bounds__(512, 2) void k_fused(const char* __restrict__ xA, const char* __restrict__ xB, const char* __restrict__ kA, const char* __restrict__ kB,
                                                   const char* __restrict__ vA, const char* __restrict__ vB, const char* __restrict__ wqA, const char* __restrict__ wqB,
                                                   const char* __restrict__ woA, const char* __restrict__ woB, char* __restrict__ Ows, char* __restrict__ out) {
  extern __shared__ __attribute__((aligned(16))) char lds[];
  const unsigned tid = threadIdx.x, lane = tid & 63u;
  const unsigned wid = __builtin_amdgcn_readfirstlane(tid >> 6);
  STAMP_DECL;
  STAMP(0); CSTAMP(60);
  const unsigned bi = blockIdx.x, xcd = bi & 7u, r = bi >> 3, br = xcd >> 2, hg = xcd & 3u, b = r & 7u, hp = r >> 3, h0 = hg * 8u + hp * 2u;
  const unsigned L0 = (unsigned)(size_t)lds;
  const rsrc_t rx = mkrsrc((br ? xB : xA) + (size_t)b * SQ * DM * 2, SQ * DM * 2);
  const rsrc_t rw = mkrsrc(br ? wqB : wqA, DM * NH * DH * 2);
  const rsrc_t rk = mkrsrc((br ? kB : kA) + (size_t)(b * NH + h0) * SK * DH * 2, 2 * SK * DH * 2);
  const rsrc_t rv = mkrsrc((br ? vB : vA) + (size_t)(b * NH + h0) * SK * DH * 2, 2 * SK * DH * 2);
  const unsigned l15 = lane & 15u, l31 = lane & 31u, hi = lane >> 5, g4 = lane >> 4, blk = g4 & 1u, qq = l15 >> 2, pp = lane & 3u;
  const unsigned hsel = wid >> 2, qb = wid & 3u;

  unsigned vx[2], vw[2];
#pragma unroll
  for (unsigned j = 0; j < 2; ++j) {
    vx[j] = (lane >> 3) * 1152u + (((lane & 7u) ^ (4u * j + g4)) << 4) + j * (9216u - 1024u);
    vw[j] = g4 * 8192u + ((l15 ^ (g4 << 2)) << 4) + j * (32768u - 1024u);
  }
  const unsigned vv = g4 * 256u + ((l15 ^ (g4 << 2)) << 4);
  auto vk = [&](unsigned p) __attribute__((always_inline)) { return g4 * 256u + ((l15 ^ ((4u * (p & 3u) + g4) & 15u)) << 4); };
  const unsigned vkA = vk(2u * wid), vkB = vk(2u * wid + 1u);
  unsigned w2k = wid * 2048u;
  auto kv_piece = [&](bool isV, unsigned head, unsigned t, unsigned slot, int j) __attribute__((always_inline)) {
    const unsigned so = head * 131072u + t * 16384u + w2k, la = L0 + slot * SLOT + w2k;
    if (isV) dma16i<KV_NT>(j, la, vv, rv, so);
    else dma16i<KV_NT>(j, la, j ? vkB : vkA, rk, so);
  };

  const unsigned rot = b + 2u * hp + 7u;
  auto stage1 = [&](int i, int j) __attribute__((always_inline)) {
    const unsigned ks = i == 0 ? 0u : 1u + ((rot + (unsigned)i) & 7u);
    const unsigned buf = L0 + (unsigned)((i % 3) * 3) * SLOT + w2k;
    if (j < 2) dma16i<W_NT>(j, buf, vx[j], rx, wid * 18432u + ks * 128u);
    else { const unsigned h = (j >> 1) - 1u; dma16i<W_NT>(j & 1, buf + (1u + h) * SLOT, vw[j & 1], rw, (ks * 64u + 8u * wid) * 8192u + (h0 + h) * 256u); }
  };
  auto issueA = [&](int i, int j) __attribute__((always_inline)) {
    if (i == -1) { if (j >= 4 && j < 8) stage1(2, j - 4); return; }
    if (i <= 5) { if (j < 6) stage1(i + 3, j); }
  };
  STAMP(48);
#pragma unroll
  for (int j = 0; j < 6; ++j) stage1(0, j);
  STAMP(49);
  asm volatile("s_barrier" ::: "memory");
#pragma unroll
  for (int j = 0; j < 6; ++j) stage1(1, j);
  STAMP(50);
  f32x16 acc[4] = {};
  const unsigned xrow = 32u * qb + l31;
  unsigned ax[4], aw[4];
#pragma unroll
  for (unsigned ks = 0; ks < 4; ++ks) ax[ks] = L0 + xrow * 128u + (((2u * ks + hi) ^ ((xrow >> 1) & 7u)) << 4);
#pragma unroll
  for (unsigned db = 0; db < 4; ++db) aw[db] = L0 + 256u * (8u * hi + qq) + (((4u * db + 2u * blk + (pp & 1u)) ^ (qq << 2)) << 4) + 8u * (pp >> 1) + (1u + hsel) * SLOT;
  {
    bf16x8 fx[2], fa[2][4];
    auto rdA = [&](int i, int ks, int db) __attribute__((always_inline)) {
      const unsigned buf = (unsigned)((i % 3) * 3) * SLOT;
      if (db == 0) fx[ks & 1] = lds_b128(buf + ax[ks]);
      fa[ks & 1][db] = cat(lds_tr(buf + aw[db] + 256u * (16u * ks)), lds_tr(buf + aw[db] + 256u * (16u * ks + 4u)));
    };
    WAITV(6);
    BARRIER();
    STAMP(1); CSTAMP(54);
#pragma unroll
    for (int db = 0; db < 4; ++db) rdA(0, 0, db);
#pragma unroll
    for (int i = 0; i < 9; ++i) {
#pragma unroll
      for (int ks = 0; ks < 4; ++ks) {
        if (ks == 3 && i < 8) {
          WAITV(6);
          BARRIER();
          STAMP(2 + i); if (i == 7) CSTAMP(55);
        }
#pragma unroll
        for (int db = 0; db < 4; ++db) {
          SB();
          acc[db] = MFMA32(fa[ks & 1][db], fx[ks & 1], acc[db]);
          SB();
          if (ks < 3) rdA(i, ks + 1, db); else if (i < 8) rdA(i + 1, 0, db);
          SB();
          if (ks == 3) issueA(i, db); else if (ks == 0) issueA(i - 1, 4 + db);
          if (i == 0 && ks == 1 && db < 2) stage1(2, 4 + db);
          if (i == 6 && ks == 3) { if (db < 2) kv_piece(false, 0, 0, 9, db); else kv_piece(false, 1, 0, 1, db - 2); }
          if (i == 7 && ks == 0 && db < 2) kv_piece(true, 0, 0, 2, db);
          if (i == 7 && ks == 3) { if (db < 2) kv_piece(false, 0, 1, 3, db); else kv_piece(true, 1, 0, 4, db - 2); }
        }
        SB();
      }
    }
  }
  bf16x8 qf[8];
#pragma unroll
  for (int s = 0; s < 8; ++s) {
    u32x4 w;
#pragma unroll
    for (int e = 0; e < 4; ++e) w[e] = cvtpk(acc[s >> 1][8 * (s & 1) + 2 * e] * CSC, acc[s >> 1][8 * (s & 1) + 2 * e + 1] * CSC);
    qf[s] = __builtin_bit_cast(bf16x8, w);
  }

  STAMP(10); CSTAMP(56);
  w2k = opq(wid * 2048u);
  unsigned ak[8], av[4];
#pragma unroll
  for (unsigned s = 0; s < 8; ++s) ak[s] = 256u * l31 + (((2u * s + hi) ^ l15) << 4);
#pragma unroll
  for (unsigned db = 0; db < 4; ++db) av[db] = 256u * (4u * hi + qq) + (((4u * db + 2u * blk + (pp >> 1)) ^ (qq << 2)) << 4) + 8u * (pp & 1u);
  f32x16 o[4] = {};
  float m = 0.f, l = 0.f;
  f32x16 S[2], negm = {};
  bf16x8 pf[4];
  unsigned sn = 5u;
  auto nxt = [](unsigned s_) __attribute__((always_inline)) { return s_ == 9u ? 0u : s_ + 1u; };
  unsigned s0 = 0, s1 = 0, s2 = 0, s3 = 0;
  auto slots = [&]() __attribute__((always_inline)) { s0 = sn; s1 = nxt(s0); s2 = nxt(s1); s3 = nxt(s2); sn = nxt(s3); };
  const rsrc_t rwo = mkrsrc((br ? woB : woA) + (size_t)hg * 1024u * 1152u, 1024u * 1152u);
  auto warmWo = [&]() __attribute__((always_inline)) {
#pragma unroll
    for (int i = 0; i < 4; ++i) dma16i<false>(i, L0 + s0 * SLOT + w2k, lane * 16u, rwo, r * 36864u + wid * 5120u);
    dma16<false, 0>(L0 + s1 * SLOT + w2k, lane * 16u, rwo, r * 36864u + wid * 5120u + 4096u);
  };
  auto issueB = [&](int t, int j) __attribute__((always_inline)) {
    const int q = j >> 1;
    if (j == 0 && t == 7) warmWo();
    if (q == 0) { if (t < 7) kv_piece(false, 1, t + 1, s0, j & 1); }
    else if (q == 1) { if (t < 7) kv_piece(true, 0, t + 1, s1, j & 1); }
    else if (q == 2) { if (t < 6) kv_piece(false, 0, t + 2, s2, j & 1); }
    else { if (t < 7) kv_piece(true, 1, t + 1, s3, j & 1); }
  };
  unsigned curK = 0, curV = 0;
  auto setK = [&](unsigned kb0) __attribute__((always_inline)) {
    const unsigned d = kb0 - curK; curK = kb0;
#pragma unroll
    for (int i = 0; i < 8; ++i) { ak[i] += d; asm volatile("" : "+v"(ak[i])); }
  };
  auto setV = [&](unsigned vb0) __attribute__((always_inline)) {
    const unsigned d = vb0 - curV; curV = vb0;
#pragma unroll
    for (int i = 0; i < 4; ++i) { av[i] += d; asm volatile("" : "+v"(av[i])); }
  };
  constexpr int DL = LOOKAHEAD;
  bf16x8 kf[DL], vf[DL];
  auto rdK = [&](int f) __attribute__((always_inline)) { kf[f % DL] = lds_b128(ak[f & 7] + 8192u * (f >> 3)); };
  auto rdV = [&](int f) __attribute__((always_inline)) { vf[f % DL] = cat(lds_tr(av[f & 3] + 256u * (16u * (f >> 2))), lds_tr(av[f & 3] + 256u * (16u * (f >> 2) + 8u))); };
  auto QK = [&](int t, int j0, bool nextV) __attribute__((always_inline)) {
#pragma unroll
    for (int f = 0; f < 16; ++f) {
      SB();
      if ((f & 7) == 0) S[f >> 3] = MFMA32(kf[f % DL], qf[f & 7], negm);
      else S[f >> 3] = MFMA32(kf[f % DL], qf[f & 7], S[f >> 3]);
      SB();
      if (f + DL < 16) rdK(f + DL); else if (nextV) rdV(f + DL - 16);
      SB();
      if ((f & 3) == 1) issueB(t, j0 + (f >> 2));
    }
    SB();
  };
  auto SM = [&](bool first) __attribute__((always_inline)) {
    SB(); __builtin_amdgcn_s_setprio(2); SB();
    float pm0 = max3f(S[0][0], S[0][1], S[1][0]), pm1 = max3f(S[0][2], S[0][3], S[1][1]);
    pm0 = max3f(pm0, S[1][2], S[1][3]);
#pragma unroll
    for (int e = 4; e < 16; e += 4) {
      pm0 = max3f(pm0, S[0][e], S[0][e + 1]); pm1 = max3f(pm1, S[0][e + 2], S[0][e + 3]);
      pm0 = max3f(pm0, S[1][e], S[1][e + 1]); pm1 = max3f(pm1, S[1][e + 2], S[1][e + 3]);
    }
    float pm = max2f(pm0, pm1);
    { auto rr = __builtin_amdgcn_permlane32_swap(__float_as_uint(pm), __float_as_uint(pm), false, false); pm = max2f(__uint_as_float(rr[0]), __uint_as_float(rr[1])); }
    if (first || __builtin_expect(__any(!(pm <= THR)), 0)) {
      const float dl = first ? pm : max2f(pm, 0.f);
      m += dl;
      if (!first) {
        const float al = __builtin_amdgcn_exp2f(-dl);
        l *= al;
#pragma unroll
        for (int db = 0; db < 4; ++db)
#pragma unroll
          for (int e = 0; e < 16; ++e) o[db][e] *= al;
      }
#pragma unroll
      for (int e = 0; e < 16; ++e) { S[0][e] -= dl; S[1][e] -= dl; negm[e] = -m; }
    }
    float l0 = 0.f, l1 = 0.f;
#pragma unroll
    for (int kb = 0; kb < 2; ++kb) {
#pragma unroll
      for (int e = 0; e < 16; e += 2) {
        S[kb][e] = __builtin_amdgcn_exp2f(S[kb][e]); l0 += S[kb][e];
        S[kb][e + 1] = __builtin_amdgcn_exp2f(S[kb][e + 1]); l1 += S[kb][e + 1];
      }
#pragma unroll
      for (int u = 0; u < 2; ++u) {
        u32x4 w;
#pragma unroll
        for (int e = 0; e < 4; ++e) w[e] = cvtpk(S[kb][8 * u + 2 * e], S[kb][8 * u + 2 * e + 1]);
        pf[2 * kb + u] = __builtin_bit_cast(bf16x8, w);
      }
    }
    l += l0 + l1;
    asm volatile("" : "+v"(pf[0]), "+v"(pf[1]), "+v"(pf[2]), "+v"(pf[3]), "+v"(l));
    SB(); __builtin_amdgcn_s_setprio(0); SB();
  };
  auto waitB = [&](int t) __attribute__((always_inline)) { if (t == 8) WAITV(0); else if (t == 7) WAITV(2); else WAITV(4); BARRIER(); };
  if (hsel == 0) {
    unsigned sK = 9u, sV = 2u;
    auto stepA = [&](int t, auto firstc) __attribute__((always_inline)) {
      constexpr bool FIRST = decltype(firstc)::value;
      waitB(t);
      STAMP(11 + t);
      ISTAMP(47);
      slots();
      setK(L0 + sK * SLOT);
#pragma unroll
      for (int i = 0; i < 4; ++i) {
        SB();
#pragma unroll
        for (int f = i * DL / 4; f < (i + 1) * DL / 4; ++f) rdK(f);
        SB();
        if (!FIRST) o[i] = MFMA32(vf[(12 + i) % DL], pf[3], o[i]);
      }
      SB();
      setV(L0 + sV * SLOT);
      ISTAMP(43);
      QK(t, 0, true);
      ISTAMP(44);
      SM(FIRST);
      ISTAMP(45);
#pragma unroll
      for (int f = 0; f < 12; ++f) {
        SB();
        o[f & 3] = MFMA32(vf[f % DL], pf[f >> 2], o[f & 3]);
        SB();
        if (f + DL < 16) rdV(f + DL);
        SB();
        if ((f & 3) == 1) issueB(t, 4 + (f >> 2));
        if (f == 11) issueB(t, 7);
      }
      SB();
      ISTAMP(46);
      sK += 4u; if (sK >= 10u) sK -= 10u;
      sV += 4u; if (sV >= 10u) sV -= 10u;
    };
    stepA(0, std::true_type{});
#pragma unroll 1
    for (int t = 1; t < 8; ++t) stepA(t, std::false_type{});
    waitB(8);
    STAMP(19);
#pragma unroll
    for (int i = 0; i < 4; ++i) o[i] = MFMA32(vf[(12 + i) % DL], pf[3], o[i]);
  } else {
    unsigned sK = 1u, sV = 0u;
    auto stepB = [&](int t, auto firstc, auto lastc) __attribute__((always_inline)) {
      constexpr bool FIRST = decltype(firstc)::value, LAST = decltype(lastc)::value;
      waitB(t);
      STAMP(11 + t);
      if (!LAST) ISTAMP(47);
      slots();
      setV(L0 + sV * SLOT);
#pragma unroll
      for (int f = 0; f < DL; ++f) rdV(f);
      if (!LAST) setK(L0 + sK * SLOT);
      SB();
      if (!LAST) ISTAMP(43);
      if (!LAST) SM(FIRST);
      if (!LAST) ISTAMP(44);
#pragma unroll
      for (int f = 0; f < 16; ++f) {
        SB();
        o[f & 3] = MFMA32(vf[f % DL], pf[f >> 2], o[f & 3]);
        SB();
        if (f + DL < 16) rdV(f + DL); else if (!LAST) rdK(f + DL - 16);
        SB();
        if ((f & 3) == 1 && !LAST) issueB(t, f >> 2);
      }
      if (!LAST) ISTAMP(45);
      if (!LAST) QK(t, 4, false);
      SB();
      if (!LAST) ISTAMP(46);
      sK += 4u; if (sK >= 10u) sK -= 10u;
      sV += 4u; if (sV >= 10u) sV -= 10u;
    };
    {
      waitB(0);
      STAMP(11);
      slots();
      setK(L0 + sK * SLOT);
#pragma unroll
      for (int f = 0; f < DL; ++f) rdK(f);
#pragma unroll
      for (int j = 0; j < 4; ++j) issueB(0, j);
      QK(0, 4, false);
      sK = 5u; sV = 4u;
    }
    stepB(1, std::true_type{}, std::false_type{});
#pragma unroll 1
    for (int t = 2; t < 8; ++t) stepB(t, std::false_type{}, std::false_type{});
    SM(false);
    stepB(8, std::false_type{}, std::true_type{});
  }
  STAMP(27); CSTAMP(57);
  l = swapsum(l);
  const float inv = __builtin_amdgcn_rcpf(l);
  const rsrc_t rob = mkrsrc(Ows + (size_t)((br * NB + b) * NH + (h0 + hsel)) * 32768u, 32768u);
  const unsigned obo = (2u * qb + (l31 >> 4)) * 4096u + (l15 + 16u * hi) * 16u;
  auto ochunk = [&](unsigned c) __attribute__((always_inline)) { return L0 + (c < 3u ? 49152u + 8192u * c : 110592u + 8192u * (c - 3u)); };
  const unsigned olds = ochunk(wid) + (l31 >> 4) * 4096u + (l15 + 16u * hi) * 16u;
#pragma unroll
  for (int db = 0; db < 4; ++db)
#pragma unroll
    for (int u = 0; u < 2; ++u) {
      u32x4 w;
#pragma unroll
      for (int e = 0; e < 4; ++e) w[e] = cvtpk(o[db][8 * u + 2 * e] * inv, o[db][8 * u + 2 * e + 1] * inv);
      __builtin_amdgcn_raw_buffer_store_b128(w, rob, obo + db * 1024 + u * 512, 0, 16);
      *(LAS u32x4*)(size_t)(olds + db * 1024 + u * 512) = w;
    }
  STAMP(28); CSTAMP(61);

  {
  char* ws2 = Ows;
  const unsigned j = hp, kh = hsel;
  STAMP(32); CSTAMP(62);
  const rsrc_t rw = mkrsrc(br ? woB : woA, NH * DH * DM * 2);
  const rsrc_t ro = mkrsrc(ws2 + WS_O + (size_t)(br * NB + b) * NH * 32768u, NH * 32768u);
  unsigned vw[5];
#pragma unroll
  for (unsigned i = 0; i < 5; ++i) { const unsigned c = 64u * (qb + 4u * i) + lane; vw[i] = (c / 18u) * 1152u + (c % 18u) * 16u; }
#define ROT2J 2
  auto kpos = [&](int u) __attribute__((always_inline)) { return ((unsigned)u + 2u * j) & 7u; };
  const bool lowq = qb < 2u;
  const unsigned ldst = L0 + kh * 73728u + qb * 1024u;
  auto stage1 = [&](int u, int i) __attribute__((always_inline)) {
    if (i == 4 && !lowq) return;
    const unsigned so = ((hg * 8u + kpos(u)) * 128u + kh * 64u) * 1152u + j * 288u;
    dma16<W2_NT, 0>(ldst + (unsigned)(u & 3) * 18432u + (unsigned)i * 4096u, vw[i], rw, so);
  };
  const unsigned vo0 = lane * 16u + 2u * qb * 4096u, vo1 = vo0 + 4096u;
  bf16x8 bf[4][4];
  auto loadB = [&](int u, int f) __attribute__((always_inline)) {
    const unsigned so = (hg * 8u + kpos(u)) * 32768u + 2u * kh * 1024u;
    if (f == 0) asm volatile("buffer_load_dwordx4 %0, %1, %2, %3 offen sc1" : "=v"(bf[u & 3][0]) : "v"(vo0), "s"(ro), "s"(so) : "memory");
    if (f == 1) asm volatile("buffer_load_dwordx4 %0, %1, %2, %3 offen sc1" : "=v"(bf[u & 3][1]) : "v"(vo1), "s"(ro), "s"(so) : "memory");
    if (f == 2) asm volatile("buffer_load_dwordx4 %0, %1, %2, %3 offen offset:1024 sc1" : "=v"(bf[u & 3][2]) : "v"(vo0), "s"(ro), "s"(so) : "memory");
    if (f == 3) asm volatile("buffer_load_dwordx4 %0, %1, %2, %3 offen offset:1024 sc1" : "=v"(bf[u & 3][3]) : "v"(vo1), "s"(ro), "s"(so) : "memory");
  };
  auto issue = [&](int u, int v) __attribute__((always_inline)) {
    if (u > 7) return;
    if (v < 4) loadB(u, v); else if (v < 9) stage1(u, v - 4);
  };
  const unsigned aw = L0 + 288u * (16u * (lane >> 5) + 4u * ((lane >> 4) & 1u) + ((lane & 15u) >> 2)) + 8u * (lane & 3u) + kh * 73728u;
  f32x4 acc[9][2] = {};
  constexpr int DW = 6;
  bf16x8 fa[DW];
  auto rdW = [&](int F) __attribute__((always_inline)) {
    const int u = F / 18, e = (F % 18) / 9, nb = F % 9;
    const unsigned buf = aw + (unsigned)(u & 3) * 18432u + (unsigned)e * 9216u;
    fa[F % DW] = cat(lds_tr(buf + 32u * nb), lds_tr(buf + 288u * 8u + 32u * nb));
  };
  auto waitK2 = [&](int u, int N) __attribute__((always_inline)) {
    bf16x8* c = bf[u & 3];
    if (N == 0) WAITV(0);
    else { if (lowq) WAITV(9); else WAITV(8); }
    asm volatile("s_barrier" : "+v"(c[0]), "+v"(c[1]), "+v"(c[2]), "+v"(c[3]) :: "memory");
  };
  const unsigned long long tag = ((unsigned long long)hip_dispatch_id() + 1ull) * 0x9E3779B97F4A7C15ull ^ 0x6A09E667F3BCC908ull;
  GAS unsigned long long* fo = (GAS unsigned long long*)(ws2 + WS_FO + (size_t)((br * NB + b) * 4u + hg) * 16u * 128u);
#pragma unroll
  for (int i = 0; i < 5; ++i) stage1(0, i);
  BARRIER();
#pragma unroll
  for (int i = 0; i < 5; ++i) stage1(1, i);
#pragma unroll
  for (int u = 0; u < 2; ++u)
#pragma unroll
    for (int f = 0; f < 4; ++f) bf[u][f] = lds_b128(ochunk(4u * u + qb) + (unsigned)(f & 1) * 4096u + (2u * kh + (unsigned)(f >> 1)) * 1024u + lane * 16u);
  if (lowq) WAITV(5); else WAITV(4);
  BARRIER();
  if (wid == 0u && lane < 4u && lane != j) __hip_atomic_store(fo + (lane * 4u + j) * 16u, tag, __ATOMIC_RELAXED, __HIP_MEMORY_SCOPE_AGENT);
  STAMP(33);
  auto pollO = [&]() __attribute__((always_inline)) {
    if (wid == 0u) {
      const unsigned pl = lane & 3u;
      unsigned spins = 0;
      for (;;) {
        const unsigned long long v = pl == j ? tag : __hip_atomic_load(fo + (j * 4u + pl) * 16u, __ATOMIC_RELAXED, __HIP_MEMORY_SCOPE_AGENT);
        if (__all(v == tag) || ++spins > (1u << 18)) break;
        __builtin_amdgcn_s_sleep(1);
      }
      if (lane < 4u && lane != j) __hip_atomic_store(fo + (j * 4u + lane) * 16u, 0ull, __ATOMIC_RELAXED, __HIP_MEMORY_SCOPE_AGENT);
      WAITV(0);
    }
  };
#pragma unroll
  for (int F = 0; F < DW; ++F) rdW(F);
  sfor<144>([&](auto Fc) __attribute__((always_inline)) {
    constexpr int F = decltype(Fc)::value, u = F / 18, p = F % 18, e = p / 9, nb = p % 9;
    if constexpr (p == 18 - DW && u == 0) {
      SB(); pollO();
      if (lowq) WAITV(5); else WAITV(4);
      asm volatile("s_barrier" : "+v"(bf[1][0]), "+v"(bf[1][1]), "+v"(bf[1][2]), "+v"(bf[1][3]) :: "memory");
      STAMP(34); STAMP(52);
    }
    if constexpr (p == 18 - DW && u > 0 && u < 7) { SB(); waitK2(u + 1, u + 2 <= 7 ? 1 : 0); STAMP(34 + u); }
    SB();
    acc[nb][0] = MFMA16(fa[F % DW], bf[u & 3][2 * e], acc[nb][0]);
    acc[nb][1] = MFMA16(fa[F % DW], bf[u & 3][2 * e + 1], acc[nb][1]);
    SB();
    if constexpr (F + DW < 144) rdW(F + DW);
    SB();
    if constexpr (u == 0) { if constexpr (p >= 4 && p < 9) issue(2, p); if constexpr (p >= 18 - DW && p < 18 - DW + 4) issue(2, p - (18 - DW)); }
    else if constexpr (p < 9) issue(u + 2, p);
  });
  SB();
  BARRIER();
  STAMP(41);
  LAS f32x4* cb = (LAS f32x4*)(size_t)(L0 + wid * 9216u + lane * 16u);
  LAS f32x4* cr = (LAS f32x4*)(size_t)(L0 + (wid ^ 4u) * 9216u + lane * 16u);
#pragma unroll
  for (int nb = 0; nb < 9; ++nb) cb[nb * 64] = kh ? acc[nb][0] : acc[nb][1];
  BARRIER();
  f32x4 tot[9];
#pragma unroll
  for (int nb = 0; nb < 9; ++nb) tot[nb] = kh ? cr[nb * 64] + acc[nb][1] : acc[nb][0] + cr[nb * 64];
  STAMP(45);
  const unsigned grp = (br * NB + b) * 4u + j;
  const rsrc_t rp = mkrsrc(ws2 + WS_P + (size_t)grp * 16u * 18432u, 16u * 18432u);
  GAS unsigned long long* fp = (GAS unsigned long long*)(ws2 + WS_FP + (size_t)grp * 16u * 128u);
  LAS f32x4* own = (LAS f32x4*)(size_t)(L0 + 73728u + lane * 16u);
  if (qb != hg) {
#pragma unroll
    for (int nb = 0; nb < 9; ++nb)
      __builtin_amdgcn_raw_buffer_store_b128(__builtin_bit_cast(u32x4, tot[nb]), rp, lane * 16u + (unsigned)nb * 1024u, (qb * 4u + hg) * 18432u + kh * 9216u, 16);
    STAMP(46);
    WAITV(0);
    STAMP(47);
  } else {
#pragma unroll
    for (int nb = 0; nb < 9; ++nb) own[(kh * 9u + nb) * 64] = tot[nb];
  }
  BARRIER();
  if (wid == 0u) {
    if (lane < 4u && lane != hg) __hip_atomic_store(fp + (lane * 4u + hg) * 16u, tag, __ATOMIC_RELAXED, __HIP_MEMORY_SCOPE_AGENT);
    const unsigned pl = lane & 3u;
    unsigned spins = 0;
    for (;;) {
      const unsigned long long v = pl == hg ? tag : __hip_atomic_load(fp + (hg * 4u + pl) * 16u, __ATOMIC_RELAXED, __HIP_MEMORY_SCOPE_AGENT);
      if (__all(v == tag) || ++spins > (1u << 18)) break;
      __builtin_amdgcn_s_sleep(1);
    }
    if (lane < 4u && lane != hg) __hip_atomic_store(fp + (hg * 4u + lane) * 16u, 0ull, __ATOMIC_RELAXED, __HIP_MEMORY_SCOPE_AGENT);
  }
  STAMP(43);
  BARRIER();
  STAMP(44);
  {
    u32x4 pr[3][3];
    const bool third = wid < 2u;
#pragma unroll
    for (int i = 0; i < 3; ++i) {
      const unsigned tau = (i < 2 || third) ? wid + 8u * i : wid;
#pragma unroll
      for (unsigned s = 0; s < 3; ++s) { const unsigned src = s + (s >= hg ? 1u : 0u); pr[i][s] = __builtin_amdgcn_raw_buffer_load_b128(rp, lane * 16u + tau * 1024u, (hg * 4u + src) * 18432u, 16); }
    }
#pragma unroll
    for (int i = 0; i < 3; ++i) {
      if (i < 2 || third) {
        const unsigned tau = wid + 8u * i;
        const f32x4 v = own[tau * 64];
        const f32x4 p0 = __builtin_bit_cast(f32x4, pr[i][0]), p1 = __builtin_bit_cast(f32x4, pr[i][1]), p2 = __builtin_bit_cast(f32x4, pr[i][2]);
        const f32x4 t0 = hg == 0 ? v : p0, t1 = hg == 0 ? p0 : (hg == 1 ? v : p1), t2 = hg <= 1 ? p1 : (hg == 2 ? v : p2), t3 = hg == 3 ? v : p2;
        const f32x4 sum = (t0 + t1) + (t2 + t3);
        const unsigned qi = tau / 9u, nb = tau % 9u;
        const unsigned q = 32u * hg + 16u * qi + (lane & 15u), n = br * 576u + j * 144u + nb * 16u + 4u * (lane >> 4);
        u32x2 w = {cvtpk(sum[0], sum[1]), cvtpk(sum[2], sum[3])};
        *(GAS u32x2*)(out + ((size_t)(b * SQ + q) * 1152u + n) * 2u) = w;
      }
    }
  }
  STAMP(42); CSTAMP(63);
  }
}

extern "C" void kernel_launch(void* const* d_in, const int* in_sizes, int n_in, void* d_out, int out_size, void* d_ws, size_t ws_size, hipStream_t stream) {
  static bool attr = false;
  if (!attr) {
    (void)hipFuncSetAttribute((const void*)k_fused, hipFuncAttributeMaxDynamicSharedMemorySize, 163840);
    attr = true;
  }
  k_fused<<<256, 512, 163840, stream>>>((const char*)d_in[0], (const char*)d_in[5], (const char*)d_in[1], (const char*)d_in[6], (const char*)d_in[2], (const char*)d_in[7],
                                        (const char*)d_in[3], (const char*)d_in[8], (const char*)d_in[4], (const char*)d_in[9], (char*)d_ws + WS_O, (char*)d_out);
}
```

```cpp
#include <hip/hip_runtime.h>
#include <stdint.h>
#include <type_traits>
#include <utility>

#define LAS __attribute__((address_space(3)))
#define GAS __attribute__((address_space(1)))
#define DI __device__ __forceinline__
typedef __attribute__((ext_vector_type(8))) __bf16 bf16x8;
typedef __attribute__((ext_vector_type(4))) __bf16 bf16x4;
typedef __attribute__((ext_vector_type(2))) __bf16 bf16x2;
typedef __attribute__((ext_vector_type(16))) float f32x16;
typedef __attribute__((ext_vector_type(4))) float f32x4;
typedef __attribute__((ext_vector_type(2))) float f32x2;
typedef __attribute__((ext_vector_type(4))) unsigned u32x4;
typedef __attribute__((ext_vector_type(2))) unsigned u32x2;
typedef __amdgpu_buffer_rsrc_t rsrc_t;

constexpr int NB = 8, SQ = 128, DM = 576, NH = 32, SK = 512, DH = 128;
constexpr int SLOT = 16384;
constexpr size_t O_BYTES = (size_t)2 * NB * NH * SQ * DH * 2;
constexpr size_t WS_O = 0, WS_P = O_BYTES;
constexpr size_t P_BYTES = (size_t)64 * 4 * 4 * 18432;
constexpr size_t WS_FO = WS_P + P_BYTES, WS_FP = WS_FO + 1024 * 128;
constexpr float CSC = 0.08838834764831845f * 1.4426950408889634f;
constexpr float THR = 8.0f;

extern "C" __device__ unsigned long long hip_dispatch_id() __asm("llvm.amdgcn.dispatch.id");
DI unsigned cvtpk(float lo, float hi) { f32x2 v = {lo, hi}; bf16x2 b = __builtin_convertvector(v, bf16x2); return __builtin_bit_cast(unsigned, b); }
DI rsrc_t mkrsrc(const void* p, unsigned bytes) { return __builtin_amdgcn_make_buffer_rsrc((void*)p, (short)0, (int)bytes, 0x00020000); }
template <bool NT, int IMM> DI void dma16(unsigned ldsaddr, unsigned voff, rsrc_t r, unsigned soff) {
  if constexpr (NT) asm volatile("s_mov_b32 m0, %0\n\ts_nop 0\n\tbuffer_load_dwordx4 %1, %2, %3 offen offset:%c4 nt lds" ::"s"(ldsaddr), "v"(voff), "s"(r), "s"(soff), "i"(IMM) : "memory");
  else asm volatile("s_mov_b32 m0, %0\n\ts_nop 0\n\tbuffer_load_dwordx4 %1, %2, %3 offen offset:%c4 lds" ::"s"(ldsaddr), "v"(voff), "s"(r), "s"(soff), "i"(IMM) : "memory");
}
template <bool NT> DI void dma16i(int imm, unsigned ldsaddr, unsigned voff, rsrc_t r, unsigned soff) {
  switch (imm) {
    case 0: dma16<NT, 0>(ldsaddr, voff, r, soff); break;
    case 1: dma16<NT, 1024>(ldsaddr, voff, r, soff); break;
    case 2: dma16<NT, 2048>(ldsaddr, voff, r, soff); break;
    default: dma16<NT, 3072>(ldsaddr, voff, r, soff); break;
  }
}
template <int... I, class F> DI void sfor_impl(std::integer_sequence<int, I...>, F&& f) { (f(std::integral_constant<int, I>{}), ...); }
template <int N, class F> DI void sfor(F&& f) { sfor_impl(std::make_integer_sequence<int, N>{}, f); }
DI unsigned opq(unsigned x) { asm volatile("" : "+s"(x)); return x; }
#define WAITV(n) asm volatile("s_waitcnt vmcnt(" #n ")" ::: "memory")
#define BARRIER() asm volatile("s_waitcnt lgkmcnt(0)\n\ts_barrier" ::: "memory")
DI bf16x8 lds_b128(unsigned a) { return *(LAS bf16x8*)(size_t)a; }
DI bf16x4 lds_tr(unsigned a) { return __builtin_amdgcn_ds_read_tr16_b64_v4bf16((LAS bf16x4*)(size_t)a); }
DI bf16x8 cat(bf16x4 a, bf16x4 b) { return __builtin_shufflevector(a, b, 0, 1, 2, 3, 4, 5, 6, 7); }
DI float swapmax(float v) { auto r = __builtin_amdgcn_permlane32_swap(__float_as_uint(v), __float_as_uint(v), false, false); return fmaxf(__uint_as_float(r[0]), __uint_as_float(r[1])); }
DI float swapsum(float v) { auto r = __builtin_amdgcn_permlane32_swap(__float_as_uint(v), __float_as_uint(v), false, false); return __uint_as_float(r[0]) + __uint_as_float(r[1]); }
#define SB() __builtin_amdgcn_sched_barrier(0)
DI float max2f(float a, float b) { return __builtin_elementwise_maximum(a, b); }
DI float max3f(float a, float b, float c) { return max2f(max2f(a, b), c); }
#define MFMA32(a, b, c) __builtin_amdgcn_mfma_f32_32x32x16_bf16(a, b, c, 0, 0, 0)
#define MFMA16(a, b, c) __builtin_amdgcn_mfma_f32_16x16x32_bf16(a, b, c, 0, 0, 0)

#define STAMP(i) do {} while (0)
#define CSTAMP(i) do {} while (0)
#define STAMP_DECL do {} while (0)
#define ISTAMP(i) do {} while (0)
#define STAMP_FLUSH(base, lo, hi) do {} while (0)
#define KV_NT true
#define W_NT false
#define LOOKAHEAD 8
#define W2_NT false
#define KVSTART 1

__global__ __launch_bounds__(512, 2) void k_fused(const char* __restrict__ xA, const char* __restrict__ xB, const char* __restrict__ kA, const char* __restrict__ kB,
                                                   const char* __restrict__ vA, const char* __restrict__ vB, const char* __restrict__ wqA, const char* __restrict__ wqB,
                                                   const char* __restrict__ woA, const char* __restrict__ woB, char* __restrict__ Ows, char* __restrict__ out) {
  extern __shared__ __attribute__((aligned(16))) char lds[];
  const unsigned tid = threadIdx.x, lane = tid & 63u;
  const unsigned wid = __builtin_amdgcn_readfirstlane(tid >> 6);
  STAMP_DECL;
  STAMP(0); CSTAMP(60);
  const unsigned bi = blockIdx.x, xcd = bi & 7u, r = bi >> 3, br = xcd >> 2, hg = xcd & 3u, b = r & 7u, hp = r >> 3, h0 = hg * 8u + hp * 2u;
  const unsigned L0 = (unsigned)(size_t)lds;
  const rsrc_t rx = mkrsrc((br ? xB : xA) + (size_t)b * SQ * DM * 2, SQ * DM * 2);
  const rsrc_t rw = mkrsrc(br ? wqB : wqA, DM * NH * DH * 2);
  const rsrc_t rk = mkrsrc((br ? kB : kA) + (size_t)(b * NH + h0) * SK * DH * 2, 2 * SK * DH * 2);
  const rsrc_t rv = mkrsrc((br ? vB : vA) + (size_t)(b * NH + h0) * SK * DH * 2, 2 * SK * DH * 2);
  const unsigned l15 = lane & 15u, l31 = lane & 31u, hi = lane >> 5, g4 = lane >> 4, blk = g4 & 1u, qq = l15 >> 2, pp = lane & 3u;
  const unsigned hsel = wid >> 2, qb = wid & 3u;

  unsigned vx[2], vw[2];
#pragma unroll
  for (unsigned j = 0; j < 2; ++j) {
    vx[j] = (lane >> 3) * 1152u + (((lane & 7u) ^ (4u * j + g4)) << 4) + j * (9216u - 1024u);
    vw[j] = g4 * 8192u + ((l15 ^ (g4 << 2)) << 4) + j * (32768u - 1024u);
  }
  const unsigned vv = g4 * 256u + ((l15 ^ (g4 << 2)) << 4);
  auto vk = [&](unsigned p) __attribute__((always_inline)) { return g4 * 256u + ((l15 ^ ((4u * (p & 3u) + g4) & 15u)) << 4); };
  const unsigned vkA = vk(2u * wid), vkB = vk(2u * wid + 1u);
  unsigned w2k = wid * 2048u;
  auto kv_piece = [&](bool isV, unsigned head, unsigned t, unsigned slot, int j) __attribute__((always_inline)) {
    const unsigned so = head * 131072u + t * 16384u + w2k, la = L0 + slot * SLOT + w2k;
    if (isV) dma16i<KV_NT>(j, la, vv, rv, so);
    else dma16i<KV_NT>(j, la, j ? vkB : vkA, rk, so);
  };

  const unsigned rot = b + 2u * hp + 7u;
  auto stage1 = [&](int i, int j) __attribute__((always_inline)) {
    const unsigned ks = i == 0 ? 0u : 1u + ((rot + (unsigned)i) & 7u);
    const unsigned buf = L0 + (unsigned)((i % 3) * 3) * SLOT + w2k;
    if (j < 2) dma16i<W_NT>(j, buf, vx[j], rx, wid * 18432u + ks * 128u);
    else { const unsigned h = (j >> 1) - 1u; dma16i<W_NT>(j & 1, buf + (1u + h) * SLOT, vw[j & 1], rw, (ks * 64u + 8u * wid) * 8192u + (h0 + h) * 256u); }
  };
  auto issueA = [&](int i, int j) __attribute__((always_inline)) {
    if (i == -1) { if (j >= 4 && j < 8) stage1(2, j - 4); return; }
    if (i <= 5) { if (j < 6) stage1(i + 3, j); }
  };
  unsigned ipf = 0;
  if (wid == 5u) {
    unsigned long long pc;
    asm volatile("s_getpc_b64 %0" : "=s"(pc));
    const char* cp = (const char*)(pc & ~0xFFFull) + (r & 7u) * 4608u + (lane < 36u ? lane : 35u) * 128u;
    asm volatile("global_load_dword %0, %1, off" : "=v"(ipf) : "v"(cp) : "memory");
  }
  STAMP(48);
#pragma unroll
  for (int j = 0; j < 6; ++j) stage1(0, j);
  STAMP(49);
  asm volatile("s_barrier" ::: "memory");
#pragma unroll
  for (int j = 0; j < 6; ++j) stage1(1, j);
  STAMP(50);
  f32x16 acc[4] = {};
  const unsigned xrow = 32u * qb + l31;
  unsigned ax[4], aw[4];
#pragma unroll
  for (unsigned ks = 0; ks < 4; ++ks) ax[ks] = L0 + xrow * 128u + (((2u * ks + hi) ^ ((xrow >> 1) & 7u)) << 4);
#pragma unroll
  for (unsigned db = 0; db < 4; ++db) aw[db] = L0 + 256u * (8u * hi + qq) + (((4u * db + 2u * blk + (pp & 1u)) ^ (qq << 2)) << 4) + 8u * (pp >> 1) + (1u + hsel) * SLOT;
  {
    bf16x8 fx[2], fa[2][4];
    auto rdA = [&](int i, int ks, int db) __attribute__((always_inline)) {
      const unsigned buf = (unsigned)((i % 3) * 3) * SLOT;
      if (db == 0) fx[ks & 1] = lds_b128(buf + ax[ks]);
      fa[ks & 1][db] = cat(lds_tr(buf + aw[db] + 256u * (16u * ks)), lds_tr(buf + aw[db] + 256u * (16u * ks + 4u)));
    };
    WAITV(6);
    asm volatile("" :: "v"(ipf));
    BARRIER();
    STAMP(1); CSTAMP(54);
#pragma unroll
    for (int db = 0; db < 4; ++db) rdA(0, 0, db);
#pragma unroll
    for (int i = 0; i < 9; ++i) {
#pragma unroll
      for (int ks = 0; ks < 4; ++ks) {
        if (ks == 3 && i < 8) {
          WAITV(6);
          BARRIER();
          STAMP(2 + i); if (i == 7) CSTAMP(55);
        }
#pragma unroll
        for (int db = 0; db < 4; ++db) {
          SB();
          acc[db] = MFMA32(fa[ks & 1][db], fx[ks & 1], acc[db]);
          SB();
          if (ks < 3) rdA(i, ks + 1, db); else if (i < 8) rdA(i + 1, 0, db);
          SB();
          if (ks == 3) issueA(i, db); else if (ks == 0) issueA(i - 1, 4 + db);
          if (i == 0 && ks == 1 && db < 2) stage1(2, 4 + db);
          if (i == 6 && ks == 3) { if (db < 2) kv_piece(false, 0, 0, 9, db); else kv_piece(false, 1, 0, 1, db - 2); }
          if (i == 7 && ks == 0 && db < 2) kv_piece(true, 0, 0, 2, db);
          if (i == 7 && ks == 3) { if (db < 2) kv_piece(false, 0, 1, 3, db); else kv_piece(true, 1, 0, 4, db - 2); }
        }
        SB();
      }
    }
  }
  bf16x8 qf[8];
#pragma unroll
  for (int s = 0; s < 8; ++s) {
    u32x4 w;
#pragma unroll
    for (int e = 0; e < 4; ++e) w[e] = cvtpk(acc[s >> 1][8 * (s & 1) + 2 * e] * CSC, acc[s >> 1][8 * (s & 1) + 2 * e + 1] * CSC);
    qf[s] = __builtin_bit_cast(bf16x8, w);
  }

  STAMP(10); CSTAMP(56);
  w2k = opq(wid * 2048u);
  unsigned ak[8], av[4];
#pragma unroll
  for (unsigned s = 0; s < 8; ++s) ak[s] = 256u * l31 + (((2u * s + hi) ^ l15) << 4);
#pragma unroll
  for (unsigned db = 0; db < 4; ++db) av[db] = 256u * (4u * hi + qq) + (((4u * db + 2u * blk + (pp >> 1)) ^ (qq << 2)) << 4) + 8u * (pp & 1u);
  f32x16 o[4] = {};
  float m = 0.f, l = 0.f;
  f32x16 S[2], negm = {};
  bf16x8 pf[4];
  unsigned sn = 5u;
  auto nxt = [](unsigned s_) __attribute__((always_inline)) { return s_ == 9u ? 0u : s_ + 1u; };
  unsigned s0 = 0, s1 = 0, s2 = 0, s3 = 0;
  auto slots = [&]() __attribute__((always_inline)) { s0 = sn; s1 = nxt(s0); s2 = nxt(s1); s3 = nxt(s2); sn = nxt(s3); };
  const rsrc_t rwo = mkrsrc((br ? woB : woA) + (size_t)hg * 1024u * 1152u, 1024u * 1152u);
  auto warmWo = [&]() __attribute__((always_inline)) {
#pragma unroll
    for (int i = 0; i < 4; ++i) dma16i<false>(i, L0 + s0 * SLOT + w2k, lane * 16u, rwo, r * 36864u + wid * 5120u);
    dma16<false, 0>(L0 + s1 * SLOT + w2k, lane * 16u, rwo, r * 36864u + wid * 5120u + 4096u);
  };
  auto issueB = [&](int t, int j) __attribute__((always_inline)) {
    const int q = j >> 1;
    if (j == 0 && t == 7) warmWo();
    if (q == 0) { if (t < 7) kv_piece(false, 1, t + 1, s0, j & 1); }
    else if (q == 1) { if (t < 7) kv_piece(true, 0, t + 1, s1, j & 1); }
    else if (q == 2) { if (t < 6) kv_piece(false, 0, t + 2, s2, j & 1); }
    else { if (t < 7) kv_piece(true, 1, t + 1, s3, j & 1); }
  };
  unsigned curK = 0, curV = 0;
  auto setK = [&](unsigned kb0) __attribute__((always_inline)) {
    const unsigned d = kb0 - curK; curK = kb0;
#pragma unroll
    for (int i = 0; i < 8; ++i) { ak[i] += d; asm volatile("" : "+v"(ak[i])); }
  };
  auto setV = [&](unsigned vb0) __attribute__((always_inline)) {
    const unsigned d = vb0 - curV; curV = vb0;
#pragma unroll
    for (int i = 0; i < 4; ++i) { av[i] += d; asm volatile("" : "+v"(av[i])); }
  };
  constexpr int DL = LOOKAHEAD;
  bf16x8 kf[DL], vf[DL];
  auto rdK = [&](int f) __attribute__((always_inline)) { kf[f % DL] = lds_b128(ak[f & 7] + 8192u * (f >> 3)); };
  auto rdV = [&](int f) __attribute__((always_inline)) { vf[f % DL] = cat(lds_tr(av[f & 3] + 256u * (16u * (f >> 2))), lds_tr(av[f & 3] + 256u * (16u * (f >> 2) + 8u))); };
  auto QK = [&](int t, int j0, bool nextV) __attribute__((always_inline)) {
#pragma unroll
    for (int f = 0; f < 16; ++f) {
      SB();
      if ((f & 7) == 0) S[f >> 3] = MFMA32(kf[f % DL], qf[f & 7], negm);
      else S[f >> 3] = MFMA32(kf[f % DL], qf[f & 7], S[f >> 3]);
      SB();
      if (f + DL < 16) rdK(f + DL); else if (nextV) rdV(f + DL - 16);
      SB();
      if ((f & 3) == 1) issueB(t, j0 + (f >> 2));
    }
    SB();
  };
  auto SM = [&](bool first) __attribute__((always_inline)) {
    SB(); __builtin_amdgcn_s_setprio(2); SB();
    float pm0 = max3f(S[0][0], S[0][1], S[1][0]), pm1 = max3f(S[0][2], S[0][3], S[1][1]);
    pm0 = max3f(pm0, S[1][2], S[1][3]);
#pragma unroll
    for (int e = 4; e < 16; e += 4) {
      pm0 = max3f(pm0, S[0][e], S[0][e + 1]); pm1 = max3f(pm1, S[0][e + 2], S[0][e + 3]);
      pm0 = max3f(pm0, S[1][e], S[1][e + 1]); pm1 = max3f(pm1, S[1][e + 2], S[1][e + 3]);
    }
    float pm = max2f(pm0, pm1);
    { auto rr = __builtin_amdgcn_permlane32_swap(__float_as_uint(pm), __float_as_uint(pm), false, false); pm = max2f(__uint_as_float(rr[0]), __uint_as_float(rr[1])); }
    if (first || __builtin_expect(__any(!(pm <= THR)), 0)) {
      const float dl = first ? pm : max2f(pm, 0.f);
      m += dl;
      if (!first) {
        const float al = __builtin_amdgcn_exp2f(-dl);
        l *= al;
#pragma unroll
        for (int db = 0; db < 4; ++db)
#pragma unroll
          for (int e = 0; e < 16; ++e) o[db][e] *= al;
      }
#pragma unroll
      for (int e = 0; e < 16; ++e) { S[0][e] -= dl; S[1][e] -= dl; negm[e] = -m; }
    }
    float l0 = 0.f, l1 = 0.f;
#pragma unroll
    for (int kb = 0; kb < 2; ++kb) {
#pragma unroll
      for (int e = 0; e < 16; e += 2) {
        S[kb][e] = __builtin_amdgcn_exp2f(S[kb][e]); l0 += S[kb][e];
        S[kb][e + 1] = __builtin_amdgcn_exp2f(S[kb][e + 1]); l1 += S[kb][e + 1];
      }
#pragma unroll
      for (int u = 0; u < 2; ++u) {
        u32x4 w;
#pragma unroll
        for (int e = 0; e < 4; ++e) w[e] = cvtpk(S[kb][8 * u + 2 * e], S[kb][8 * u + 2 * e + 1]);
        pf[2 * kb + u] = __builtin_bit_cast(bf16x8, w);
      }
    }
    l += l0 + l1;
    asm volatile("" : "+v"(pf[0]), "+v"(pf[1]), "+v"(pf[2]), "+v"(pf[3]), "+v"(l));
    SB(); __builtin_amdgcn_s_setprio(0); SB();
  };
  auto waitB = [&](int t) __attribute__((always_inline)) { if (t == 8) WAITV(0); else if (t == 7) WAITV(2); else WAITV(4); BARRIER(); };
  if (hsel == 0) {
    unsigned sK = 9u, sV = 2u;
    auto stepA = [&](int t, auto firstc) __attribute__((always_inline)) {
      constexpr bool FIRST = decltype(firstc)::value;
      waitB(t);
      STAMP(11 + t);
      ISTAMP(47);
      slots();
      setK(L0 + sK * SLOT);
#pragma unroll
      for (int i = 0; i < 4; ++i) {
        SB();
#pragma unroll
        for (int f = i * DL / 4; f < (i + 1) * DL / 4; ++f) rdK(f);
        SB();
        if (!FIRST) o[i] = MFMA32(vf[(12 + i) % DL], pf[3], o[i]);
      }
      SB();
      setV(L0 + sV * SLOT);
      ISTAMP(43);
      QK(t, 0, true);
      ISTAMP(44);
      SM(FIRST);
      ISTAMP(45);
#pragma unroll
      for (int f = 0; f < 12; ++f) {
        SB();
        o[f & 3] = MFMA32(vf[f % DL], pf[f >> 2], o[f & 3]);
        SB();
        if (f + DL < 16) rdV(f + DL);
        SB();
        if ((f & 3) == 1) issueB(t, 4 + (f >> 2));
        if (f == 11) issueB(t, 7);
      }
      SB();
      ISTAMP(46);
      sK += 4u; if (sK >= 10u) sK -= 10u;
      sV += 4u; if (sV >= 10u) sV -= 10u;
    };
    stepA(0, std::true_type{});
#pragma unroll 1
    for (int t = 1; t < 8; ++t) stepA(t, std::false_type{});
    waitB(8);
    STAMP(19);
#pragma unroll
    for (int i = 0; i < 4; ++i) o[i] = MFMA32(vf[(12 + i) % DL], pf[3], o[i]);
  } else {
    unsigned sK = 1u, sV = 0u;
    auto stepB = [&](int t, auto firstc, auto lastc) __attribute__((always_inline)) {
      constexpr bool FIRST = decltype(firstc)::value, LAST = decltype(lastc)::value;
      waitB(t);
      STAMP(11 + t);
      if (!LAST) ISTAMP(47);
      slots();
      setV(L0 + sV * SLOT);
#pragma unroll
      for (int f = 0; f < DL; ++f) rdV(f);
      if (!LAST) setK(L0 + sK * SLOT);
      SB();
      if (!LAST) ISTAMP(43);
      if (!LAST) SM(FIRST);
      if (!LAST) ISTAMP(44);
#pragma unroll
      for (int f = 0; f < 16; ++f) {
        SB();
        o[f & 3] = MFMA32(vf[f % DL], pf[f >> 2], o[f & 3]);
        SB();
        if (f + DL < 16) rdV(f + DL); else if (!LAST) rdK(f + DL - 16);
        SB();
        if ((f & 3) == 1 && !LAST) issueB(t, f >> 2);
      }
      if (!LAST) ISTAMP(45);
      if (!LAST) QK(t, 4, false);
      SB();
      if (!LAST) ISTAMP(46);
      sK += 4u; if (sK >= 10u) sK -= 10u;
      sV += 4u; if (sV >= 10u) sV -= 10u;
    };
    {
      waitB(0);
      STAMP(11);
      slots();
      setK(L0 + sK * SLOT);
#pragma unroll
      for (int f = 0; f < DL; ++f) rdK(f);
#pragma unroll
      for (int j = 0; j < 4; ++j) issueB(0, j);
      QK(0, 4, false);
      sK = 5u; sV = 4u;
    }
    stepB(1, std::true_type{}, std::false_type{});
#pragma unroll 1
    for (int t = 2; t < 8; ++t) stepB(t, std::false_type{}, std::false_type{});
    SM(false);
    stepB(8, std::false_type{}, std::true_type{});
  }
  STAMP(27); CSTAMP(57);
  l = swapsum(l);
  const float inv = __builtin_amdgcn_rcpf(l);
  const rsrc_t rob = mkrsrc(Ows + (size_t)((br * NB + b) * NH + (h0 + hsel)) * 32768u, 32768u);
  const unsigned obo = (2u * qb + (l31 >> 4)) * 4096u + (l15 + 16u * hi) * 16u;
  auto ochunk = [&](unsigned c) __attribute__((always_inline)) { return L0 + (c < 3u ? 49152u + 8192u * c : 110592u + 8192u * (c - 3u)); };
  const unsigned olds = ochunk(wid) + (l31 >> 4) * 4096u + (l15 + 16u * hi) * 16u;
#pragma unroll
  for (int db = 0; db < 4; ++db)
#pragma unroll
    for (int u = 0; u < 2; ++u) {
      u32x4 w;
#pragma unroll
      for (int e = 0; e < 4; ++e) w[e] = cvtpk(o[db][8 * u + 2 * e] * inv, o[db][8 * u + 2 * e + 1] * inv);
      __builtin_amdgcn_raw_buffer_store_b128(w, rob, obo + db * 1024 + u * 512, 0, 16);
      *(LAS u32x4*)(size_t)(olds + db * 1024 + u * 512) = w;
    }
  STAMP(28); CSTAMP(61);

  {
  char* ws2 = Ows;
  const unsigned j = hp, kh = hsel;
  STAMP(32); CSTAMP(62);
  const rsrc_t rw = mkrsrc(br ? woB : woA, NH * DH * DM * 2);
  const rsrc_t ro = mkrsrc(ws2 + WS_O + (size_t)(br * NB + b) * NH * 32768u, NH * 32768u);
  unsigned vw[5];
#pragma unroll
  for (unsigned i = 0; i < 5; ++i) { const unsigned c = 64u * (qb + 4u * i) + lane; vw[i] = (c / 18u) * 1152u + (c % 18u) * 16u; }
#define ROT2J 2
  auto kpos = [&](int u) __attribute__((always_inline)) { return ((unsigned)u + 2u * j) & 7u; };
  const bool lowq = qb < 2u;
  const unsigned ldst = L0 + kh * 73728u + qb * 1024u;
  auto stage1 = [&](int u, int i) __attribute__((always_inline)) {
    if (i == 4 && !lowq) return;
    const unsigned so = ((hg * 8u + kpos(u)) * 128u + kh * 64u) * 1152u + j * 288u;
    dma16<W2_NT, 0>(ldst + (unsigned)(u & 3) * 18432u + (unsigned)i * 4096u, vw[i], rw, so);
  };
  const unsigned vo0 = lane * 16u + 2u * qb * 4096u, vo1 = vo0 + 4096u;
  bf16x8 bf[4][4];
  auto loadB = [&](int u, int f) __attribute__((always_inline)) {
    const unsigned so = (hg * 8u + kpos(u)) * 32768u + 2u * kh * 1024u;
    if (f == 0) asm volatile("buffer_load_dwordx4 %0, %1, %2, %3 offen sc1" : "=v"(bf[u & 3][0]) : "v"(vo0), "s"(ro), "s"(so) : "memory");
    if (f == 1) asm volatile("buffer_load_dwordx4 %0, %1, %2, %3 offen sc1" : "=v"(bf[u & 3][1]) : "v"(vo1), "s"(ro), "s"(so) : "memory");
    if (f == 2) asm volatile("buffer_load_dwordx4 %0, %1, %2, %3 offen offset:1024 sc1" : "=v"(bf[u & 3][2]) : "v"(vo0), "s"(ro), "s"(so) : "memory");
    if (f == 3) asm volatile("buffer_load_dwordx4 %0, %1, %2, %3 offen offset:1024 sc1" : "=v"(bf[u & 3][3]) : "v"(vo1), "s"(ro), "s"(so) : "memory");
  };
  auto issue = [&](int u, int v) __attribute__((always_inline)) {
    if (u > 7) return;
    if (v < 4) loadB(u, v); else if (v < 9) stage1(u, v - 4);
  };
  const unsigned aw = L0 + 288u * (16u * (lane >> 5) + 4u * ((lane >> 4) & 1u) + ((lane & 15u) >> 2)) + 8u * (lane & 3u) + kh * 73728u;
  f32x4 acc[9][2] = {};
  constexpr int DW = 6;
  bf16x8 fa[DW];
  auto rdW = [&](int F) __attribute__((always_inline)) {
    const int u = F / 18, e = (F % 18) / 9, nb = F % 9;
    const unsigned buf = aw + (unsigned)(u & 3) * 18432u + (unsigned)e * 9216u;
    fa[F % DW] = cat(lds_tr(buf + 32u * nb), lds_tr(buf + 288u * 8u + 32u * nb));
  };
  auto waitK2 = [&](int u, int N) __attribute__((always_inline)) {
    bf16x8* c = bf[u & 3];
    if (N == 0) WAITV(0);
    else { if (lowq) WAITV(9); else WAITV(8); }
    asm volatile("s_barrier" : "+v"(c[0]), "+v"(c[1]), "+v"(c[2]), "+v"(c[3]) :: "memory");
  };
  const unsigned long long tag = ((unsigned long long)hip_dispatch_id() + 1ull) * 0x9E3779B97F4A7C15ull ^ 0x6A09E667F3BCC908ull;
  GAS unsigned long long* fo = (GAS unsigned long long*)(ws2 + WS_FO + (size_t)((br * NB + b) * 4u + hg) * 16u * 128u);
#pragma unroll
  for (int i = 0; i < 5; ++i) stage1(0, i);
  BARRIER();
#pragma unroll
  for (int i = 0; i < 5; ++i) stage1(1, i);
#pragma unroll
  for (int u = 0; u < 2; ++u)
#pragma unroll
    for (int f = 0; f < 4; ++f) bf[u][f] = lds_b128(ochunk(4u * u + qb) + (unsigned)(f & 1) * 4096u + (2u * kh + (unsigned)(f >> 1)) * 1024u + lane * 16u);
  if (lowq) WAITV(5); else WAITV(4);
  BARRIER();
  if (wid == 0u && lane < 4u && lane != j) __hip_atomic_store(fo + (lane * 4u + j) * 16u, tag, __ATOMIC_RELAXED, __HIP_MEMORY_SCOPE_AGENT);
  STAMP(33);
  auto pollO = [&]() __attribute__((always_inline)) {
    if (wid == 0u) {
      const unsigned pl = lane & 3u;
      unsigned spins = 0;
      for (;;) {
        const unsigned long long v = pl == j ? tag : __hip_atomic_load(fo + (j * 4u + pl) * 16u, __ATOMIC_RELAXED, __HIP_MEMORY_SCOPE_AGENT);
        if (__all(v == tag) || ++spins > (1u << 18)) break;
        __builtin_amdgcn_s_sleep(1);
      }
      if (lane < 4u && lane != j) __hip_atomic_store(fo + (j * 4u + lane) * 16u, 0ull, __ATOMIC_RELAXED, __HIP_MEMORY_SCOPE_AGENT);
      WAITV(0);
    }
  };
#pragma unroll
  for (int F = 0; F < DW; ++F) rdW(F);
  sfor<144>([&](auto Fc) __attribute__((always_inline)) {
    constexpr int F = decltype(Fc)::value, u = F / 18, p = F % 18, e = p / 9, nb = p % 9;
    if constexpr (p == 18 - DW && u == 0) {
      SB(); pollO();
      if (lowq) WAITV(5); else WAITV(4);
      asm volatile("s_barrier" : "+v"(bf[1][0]), "+v"(bf[1][1]), "+v"(bf[1][2]), "+v"(bf[1][3]) :: "memory");
      STAMP(34); STAMP(52);
    }
    if constexpr (p == 18 - DW && u > 0 && u < 7) { SB(); waitK2(u + 1, u + 2 <= 7 ? 1 : 0); STAMP(34 + u); }
    SB();
    acc[nb][0] = MFMA16(fa[F % DW], bf[u & 3][2 * e], acc[nb][0]);
    acc[nb][1] = MFMA16(fa[F % DW], bf[u & 3][2 * e + 1], acc[nb][1]);
    SB();
    if constexpr (F + DW < 144) rdW(F + DW);
    SB();
    if constexpr (u == 0) { if constexpr (p >= 4 && p < 9) issue(2, p); if constexpr (p >= 18 - DW && p < 18 - DW + 4) issue(2, p - (18 - DW)); }
    else if constexpr (p < 9) issue(u + 2, p);
  });
  SB();
  BARRIER();
  STAMP(41);
  LAS f32x4* cb = (LAS f32x4*)(size_t)(L0 + wid * 9216u + lane * 16u);
  LAS f32x4* cr = (LAS f32x4*)(size_t)(L0 + (wid ^ 4u) * 9216u + lane * 16u);
#pragma unroll
  for (int nb = 0; nb < 9; ++nb) cb[nb * 64] = kh ? acc[nb][0] : acc[nb][1];
  BARRIER();
  f32x4 tot[9];
#pragma unroll
  for (int nb = 0; nb < 9; ++nb) tot[nb] = kh ? cr[nb * 64] + acc[nb][1] : acc[nb][0] + cr[nb * 64];
  STAMP(45);
  const unsigned grp = (br * NB + b) * 4u + j;
  const rsrc_t rp = mkrsrc(ws2 + WS_P + (size_t)grp * 16u * 18432u, 16u * 18432u);
  GAS unsigned long long* fp = (GAS unsigned long long*)(ws2 + WS_FP + (size_t)grp * 16u * 128u);
  LAS f32x4* own = (LAS f32x4*)(size_t)(L0 + 73728u + lane * 16u);
  if (qb != hg) {
#pragma unroll
    for (int nb = 0; nb < 9; ++nb)
      __builtin_amdgcn_raw_buffer_store_b128(__builtin_bit_cast(u32x4, tot[nb]), rp, lane * 16u + (unsigned)nb * 1024u, (qb * 4u + hg) * 18432u + kh * 9216u, 16);
    STAMP(46);
    WAITV(0);
    STAMP(47);
  } else {
#pragma unroll
    for (int nb = 0; nb < 9; ++nb) own[(kh * 9u + nb) * 64] = tot[nb];
  }
  BARRIER();
  if (wid == 0u) {
    if (lane < 4u && lane != hg) __hip_atomic_store(fp + (lane * 4u + hg) * 16u, tag, __ATOMIC_RELAXED, __HIP_MEMORY_SCOPE_AGENT);
    const unsigned pl = lane & 3u;
    unsigned spins = 0;
    for (;;) {
      const unsigned long long v = pl == hg ? tag : __hip_atomic_load(fp + (hg * 4u + pl) * 16u, __ATOMIC_RELAXED, __HIP_MEMORY_SCOPE_AGENT);
      if (__all(v == tag) || ++spins > (1u << 18)) break;
      __builtin_amdgcn_s_sleep(1);
    }
    if (lane < 4u && lane != hg) __hip_atomic_store(fp + (hg * 4u + lane) * 16u, 0ull, __ATOMIC_RELAXED, __HIP_MEMORY_SCOPE_AGENT);
  }
  STAMP(43);
  BARRIER();
  STAMP(44);
  {
    u32x4 pr[3][3];
    const bool third = wid < 2u;
#pragma unroll
    for (int i = 0; i < 3; ++i) {
      const unsigned tau = (i < 2 || third) ? wid + 8u * i : wid;
#pragma unroll
      for (unsigned s = 0; s < 3; ++s) { const unsigned src = s + (s >= hg ? 1u : 0u); pr[i][s] = __builtin_amdgcn_raw_buffer_load_b128(rp, lane * 16u + tau * 1024u, (hg * 4u + src) * 18432u, 16); }
    }
#pragma unroll
    for (int i = 0; i < 3; ++i) {
      if (i < 2 || third) {
        const unsigned tau = wid + 8u * i;
        const f32x4 v = own[tau * 64];
        const f32x4 p0 = __builtin_bit_cast(f32x4, pr[i][0]), p1 = __builtin_bit_cast(f32x4, pr[i][1]), p2 = __builtin_bit_cast(f32x4, pr[i][2]);
        const f32x4 t0 = hg == 0 ? v : p0, t1 = hg == 0 ? p0 : (hg == 1 ? v : p1), t2 = hg <= 1 ? p1 : (hg == 2 ? v : p2), t3 = hg == 3 ? v : p2;
        const f32x4 sum = (t0 + t1) + (t2 + t3);
        const unsigned qi = tau / 9u, nb = tau % 9u;
        const unsigned q = 32u * hg + 16u * qi + (lane & 15u), n = br * 576u + j * 144u + nb * 16u + 4u * (lane >> 4);
        u32x2 w = {cvtpk(sum[0], sum[1]), cvtpk(sum[2], sum[3])};
        *(GAS u32x2*)(out + ((size_t)(b * SQ + q) * 1152u + n) * 2u) = w;
      }
    }
  }
  STAMP(42); CSTAMP(63);
  }
}

extern "C" void kernel_launch(void* const* d_in, const int* in_sizes, int n_in, void* d_out, int out_size, void* d_ws, size_t ws_size, hipStream_t stream) {
  static bool attr = false;
  if (!attr) {
    (void)hipFuncSetAttribute((const void*)k_fused, hipFuncAttributeMaxDynamicSharedMemorySize, 163840);
    attr = true;
  }
  k_fused<<<256, 512, 163840, stream>>>((const char*)d_in[0], (const char*)d_in[5], (const char*)d_in[1], (const char*)d_in[6], (const char*)d_in[2], (const char*)d_in[7],
                                        (const char*)d_in[3], (const char*)d_in[8], (const char*)d_in[4], (const char*)d_in[9], (char*)d_ws + WS_O, (char*)d_out);
}
```

```cpp
#include <hip/hip_runtime.h>
#include <stdint.h>
#include <type_traits>
#include <utility>

#define LAS __attribute__((address_space(3)))
#define GAS __attribute__((address_space(1)))
#define DI __device__ __forceinline__
typedef __attribute__((ext_vector_type(8))) __bf16 bf16x8;
typedef __attribute__((ext_vector_type(4))) __bf16 bf16x4;
typedef __attribute__((ext_vector_type(2))) __bf16 bf16x2;
typedef __attribute__((ext_vector_type(16))) float f32x16;
typedef __attribute__((ext_vector_type(4))) float f32x4;
typedef __attribute__((ext_vector_type(2))) float f32x2;
typedef __attribute__((ext_vector_type(4))) unsigned u32x4;
typedef __attribute__((ext_vector_type(2))) unsigned u32x2;
typedef __amdgpu_buffer_rsrc_t rsrc_t;

constexpr int NB = 8, SQ = 128, DM = 576, NH = 32, SK = 512, DH = 128;
constexpr int SLOT = 16384;
constexpr size_t O_BYTES = (size_t)2 * NB * NH * SQ * DH * 2;
constexpr size_t WS_O = 0, WS_P = O_BYTES;
constexpr size_t P_BYTES = (size_t)64 * 4 * 4 * 18432;
constexpr size_t WS_FO = WS_P + P_BYTES, WS_FP = WS_FO + 1024 * 128;
constexpr float CSC = 0.08838834764831845f * 1.4426950408889634f;
constexpr float THR = 8.0f;

extern "C" __device__ unsigned long long hip_dispatch_id() __asm("llvm.amdgcn.dispatch.id");
DI unsigned cvtpk(float lo, float hi) { f32x2 v = {lo, hi}; bf16x2 b = __builtin_convertvector(v, bf16x2); return __builtin_bit_cast(unsigned, b); }
DI rsrc_t mkrsrc(const void* p, unsigned bytes) { return __builtin_amdgcn_make_buffer_rsrc((void*)p, (short)0, (int)bytes, 0x00020000); }
template <bool NT, int IMM> DI void dma16(unsigned ldsaddr, unsigned voff, rsrc_t r, unsigned soff) {
  if constexpr (NT) asm volatile("s_mov_b32 m0, %0\n\ts_nop 0\n\tbuffer_load_dwordx4 %1, %2, %3 offen offset:%c4 nt lds" ::"s"(ldsaddr), "v"(voff), "s"(r), "s"(soff), "i"(IMM) : "memory");
  else asm volatile("s_mov_b32 m0, %0\n\ts_nop 0\n\tbuffer_load_dwordx4 %1, %2, %3 offen offset:%c4 lds" ::"s"(ldsaddr), "v"(voff), "s"(r), "s"(soff), "i"(IMM) : "memory");
}
template <bool NT> DI void dma16i(int imm, unsigned ldsaddr, unsigned voff, rsrc_t r, unsigned soff) {
  switch (imm) {
    case 0: dma16<NT, 0>(ldsaddr, voff, r, soff); break;
    case 1: dma16<NT, 1024>(ldsaddr, voff, r, soff); break;
    case 2: dma16<NT, 2048>(ldsaddr, voff, r, soff); break;
    default: dma16<NT, 3072>(ldsaddr, voff, r, soff); break;
  }
}
template <int... I, class F> DI void sfor_impl(std::integer_sequence<int, I...>, F&& f) { (f(std::integral_constant<int, I>{}), ...); }
template <int N, class F> DI void sfor(F&& f) { sfor_impl(std::make_integer_sequence<int, N>{}, f); }
DI unsigned opq(unsigned x) { asm volatile("" : "+s"(x)); return x; }
#define WAITV(n) asm volatile("s_waitcnt vmcnt(" #n ")" ::: "memory")
#define BARRIER() asm volatile("s_waitcnt lgkmcnt(0)\n\ts_barrier" ::: "memory")
DI bf16x8 lds_b128(unsigned a) { return *(LAS bf16x8*)(size_t)a; }
DI bf16x4 lds_tr(unsigned a) { return __builtin_amdgcn_ds_read_tr16_b64_v4bf16((LAS bf16x4*)(size_t)a); }
DI bf16x8 cat(bf16x4 a, bf16x4 b) { return __builtin_shufflevector(a, b, 0, 1, 2, 3, 4, 5, 6, 7); }
DI float swapmax(float v) { auto r = __builtin_amdgcn_permlane32_swap(__float_as_uint(v), __float_as_uint(v), false, false); return fmaxf(__uint_as_float(r[0]), __uint_as_float(r[1])); }
DI float swapsum(float v) { auto r = __builtin_amdgcn_permlane32_swap(__float_as_uint(v), __float_as_uint(v), false, false); return __uint_as_float(r[0]) + __uint_as_float(r[1]); }
#define SB() __builtin_amdgcn_sched_barrier(0)
DI float max2f(float a, float b) { return __builtin_elementwise_maximum(a, b); }
DI float max3f(float a, float b, float c) { return max2f(max2f(a, b), c); }
#define MFMA32(a, b, c) __builtin_amdgcn_mfma_f32_32x32x16_bf16(a, b, c, 0, 0, 0)
#define MFMA16(a, b, c) __builtin_amdgcn_mfma_f32_16x16x32_bf16(a, b, c, 0, 0, 0)

#define STAMP(i) do {} while (0)
#define CSTAMP(i) do {} while (0)
#define STAMP_DECL do {} while (0)
#define ISTAMP(i) do {} while (0)
#define STAMP_FLUSH(base, lo, hi) do {} while (0)
#define KV_NT true
#define W_NT false
#define LOOKAHEAD 8
#define W2_NT false
#define KVSTART 1

__global__ __launch_bounds__(512, 2) void k_fused(const char* __restrict__ xA, const char* __restrict__ xB, const char* __restrict__ kA, const char* __restrict__ kB,
                                                   const char* __restrict__ vA, const char* __restrict__ vB, const char* __restrict__ wqA, const char* __restrict__ wqB,
                                                   const char* __restrict__ woA, const char* __restrict__ woB, char* __restrict__ Ows, char* __restrict__ out) {
  extern __shared__ __attribute__((aligned(16))) char lds[];
  const unsigned tid = threadIdx.x, lane = tid & 63u;
  const unsigned wid = __builtin_amdgcn_readfirstlane(tid >> 6);
  STAMP_DECL;
  STAMP(0); CSTAMP(60);
  const unsigned bi = blockIdx.x, xcd = bi & 7u, r = bi >> 3, br = xcd >> 2, hg = xcd & 3u, b = r & 7u, hp = r >> 3, h0 = hg * 8u + hp * 2u;
  const unsigned L0 = (unsigned)(size_t)lds;
  const rsrc_t rx = mkrsrc((br ? xB : xA) + (size_t)b * SQ * DM * 2, SQ * DM * 2);
  const rsrc_t rw = mkrsrc(br ? wqB : wqA, DM * NH * DH * 2);
  const rsrc_t rk = mkrsrc((br ? kB : kA) + (size_t)(b * NH + h0) * SK * DH * 2, 2 * SK * DH * 2);
  const rsrc_t rv = mkrsrc((br ? vB : vA) + (size_t)(b * NH + h0) * SK * DH * 2, 2 * SK * DH * 2);
  const unsigned l15 = lane & 15u, l31 = lane & 31u, hi = lane >> 5, g4 = lane >> 4, blk = g4 & 1u, qq = l15 >> 2, pp = lane & 3u;
  const unsigned hsel = wid >> 2, qb = wid & 3u;

  unsigned vx[2], vw[2];
#pragma unroll
  for (unsigned j = 0; j < 2; ++j) {
    vx[j] = (lane >> 3) * 1152u + (((lane & 7u) ^ (4u * j + g4)) << 4) + j * (9216u - 1024u);
    vw[j] = g4 * 8192u + ((l15 ^ (g4 << 2)) << 4) + j * (32768u - 1024u);
  }
  const unsigned vv = g4 * 256u + ((l15 ^ (g4 << 2)) << 4);
  auto vk = [&](unsigned p) __attribute__((always_inline)) { return g4 * 256u + ((l15 ^ ((4u * (p & 3u) + g4) & 15u)) << 4); };
  const unsigned vkA = vk(2u * wid), vkB = vk(2u * wid + 1u);
  unsigned w2k = wid * 2048u;
  auto kv_piece = [&](bool isV, unsigned head, unsigned t, unsigned slot, int j) __attribute__((always_inline)) {
    const unsigned so = head * 131072u + t * 16384u + w2k, la = L0 + slot * SLOT + w2k;
    if (isV) dma16i<KV_NT>(j, la, vv, rv, so);
    else dma16i<KV_NT>(j, la, j ? vkB : vkA, rk, so);
  };

  const unsigned rot = b + 2u * hp + 7u;
  auto stage1 = [&](int i, int j) __attribute__((always_inline)) {
    const unsigned ks = i == 0 ? 0u : 1u + ((rot + (unsigned)i) & 7u);
    const unsigned buf = L0 + (unsigned)((i % 3) * 3) * SLOT + w2k;
    if (j < 2) dma16i<W_NT>(j, buf, vx[j], rx, wid * 18432u + ks * 128u);
    else { const unsigned h = (j >> 1) - 1u; dma16i<W_NT>(j & 1, buf + (1u + h) * SLOT, vw[j & 1], rw, (ks * 64u + 8u * wid) * 8192u + (h0 + h) * 256u); }
  };
  const rsrc_t rwo = mkrsrc((br ? woB : woA) + (size_t)hg * 1024u * 1152u, 1024u * 1152u);
  auto warmWo = [&](int i) __attribute__((always_inline)) {
    const unsigned dst = L0 + 9u * SLOT + (wid & 3u) * 4096u, so = r * 36864u + wid * 5120u;
    if (i < 4) dma16i<false>(i, dst, lane * 16u, rwo, so); else dma16<false, 0>(dst, lane * 16u, rwo, so + 4096u);
  };
  auto issueA = [&](int i, int j) __attribute__((always_inline)) {
    if (i == -1) { if (j >= 4 && j < 8) stage1(2, j - 4); return; }
    if (i <= 5) { if (j < 6) stage1(i + 3, j); }
  };
  unsigned ipf = 0;
  if (wid == 5u) {
    unsigned long long pc;
    asm volatile("s_getpc_b64 %0" : "=s"(pc));
    const char* cp = (const char*)(pc & ~0xFFFull) + (r & 7u) * 4608u + (lane < 36u ? lane : 35u) * 128u;
    asm volatile("global_load_dword %0, %1, off" : "=v"(ipf) : "v"(cp) : "memory");
  }
  STAMP(48);
#pragma unroll
  for (int j = 0; j < 6; ++j) stage1(0, j);
  STAMP(49);
  asm volatile("s_barrier" ::: "memory");
#pragma unroll
  for (int j = 0; j < 6; ++j) stage1(1, j);
  STAMP(50);
  f32x16 acc[4] = {};
  const unsigned xrow = 32u * qb + l31;
  unsigned ax[4], aw[4];
#pragma unroll
  for (unsigned ks = 0; ks < 4; ++ks) ax[ks] = L0 + xrow * 128u + (((2u * ks + hi) ^ ((xrow >> 1) & 7u)) << 4);
#pragma unroll
  for (unsigned db = 0; db < 4; ++db) aw[db] = L0 + 256u * (8u * hi + qq) + (((4u * db + 2u * blk + (pp & 1u)) ^ (qq << 2)) << 4) + 8u * (pp >> 1) + (1u + hsel) * SLOT;
  {
    bf16x8 fx[2], fa[2][4];
    auto rdA = [&](int i, int ks, int db) __attribute__((always_inline)) {
      const unsigned buf = (unsigned)((i % 3) * 3) * SLOT;
      if (db == 0) fx[ks & 1] = lds_b128(buf + ax[ks]);
      fa[ks & 1][db] = cat(lds_tr(buf + aw[db] + 256u * (16u * ks)), lds_tr(buf + aw[db] + 256u * (16u * ks + 4u)));
    };
    WAITV(6);
    asm volatile("" :: "v"(ipf));
    BARRIER();
    STAMP(1); CSTAMP(54);
#pragma unroll
    for (int db = 0; db < 4; ++db) rdA(0, 0, db);
#pragma unroll
    for (int i = 0; i < 9; ++i) {
#pragma unroll
      for (int ks = 0; ks < 4; ++ks) {
        if (ks == 3 && i < 8) {
          if (i == 1 || i == 2) WAITV(11); else WAITV(6);
          BARRIER();
          STAMP(2 + i); if (i == 7) CSTAMP(55);
        }
#pragma unroll
        for (int db = 0; db < 4; ++db) {
          SB();
          acc[db] = MFMA32(fa[ks & 1][db], fx[ks & 1], acc[db]);
          SB();
          if (ks < 3) rdA(i, ks + 1, db); else if (i < 8) rdA(i + 1, 0, db);
          SB();
          if (ks == 3) issueA(i, db); else if (ks == 0) issueA(i - 1, 4 + db);
          if (i == 0 && ks == 1 && db < 2) stage1(2, 4 + db);
          if (i == 1 && ks == 1) warmWo(db);
          if (i == 1 && ks == 2 && db == 0) warmWo(4);
          if (i == 6 && ks == 3) { if (db < 2) kv_piece(false, 0, 0, 9, db); else kv_piece(false, 1, 0, 1, db - 2); }
          if (i == 7 && ks == 0 && db < 2) kv_piece(true, 0, 0, 2, db);
          if (i == 7 && ks == 3) { if (db < 2) kv_piece(false, 0, 1, 3, db); else kv_piece(true, 1, 0, 4, db - 2); }
        }
        SB();
      }
    }
  }
  bf16x8 qf[8];
#pragma unroll
  for (int s = 0; s < 8; ++s) {
    u32x4 w;
#pragma unroll
    for (int e = 0; e < 4; ++e) w[e] = cvtpk(acc[s >> 1][8 * (s & 1) + 2 * e] * CSC, acc[s >> 1][8 * (s & 1) + 2 * e + 1] * CSC);
    qf[s] = __builtin_bit_cast(bf16x8, w);
  }

  STAMP(10); CSTAMP(56);
  w2k = opq(wid * 2048u);
  unsigned ak[8], av[4];
#pragma unroll
  for (unsigned s = 0; s < 8; ++s) ak[s] = 256u * l31 + (((2u * s + hi) ^ l15) << 4);
#pragma unroll
  for (unsigned db = 0; db < 4; ++db) av[db] = 256u * (4u * hi + qq) + (((4u * db + 2u * blk + (pp >> 1)) ^ (qq << 2)) << 4) + 8u * (pp & 1u);
  f32x16 o[4] = {};
  float m = 0.f, l = 0.f;
  f32x16 S[2], negm = {};
  bf16x8 pf[4];
  unsigned sn = 5u;
  auto nxt = [](unsigned s_) __attribute__((always_inline)) { return s_ == 9u ? 0u : s_ + 1u; };
  unsigned s0 = 0, s1 = 0, s2 = 0, s3 = 0;
  auto slots = [&]() __attribute__((always_inline)) { s0 = sn; s1 = nxt(s0); s2 = nxt(s1); s3 = nxt(s2); sn = nxt(s3); };
  auto issueB = [&](int t, int j) __attribute__((always_inline)) {
    const int q = j >> 1;
    if (q == 0) { if (t < 7) kv_piece(false, 1, t + 1, s0, j & 1); }
    else if (q == 1) { if (t < 7) kv_piece(true, 0, t + 1, s1, j & 1); }
    else if (q == 2) { if (t < 6) kv_piece(false, 0, t + 2, s2, j & 1); }
    else { if (t < 7) kv_piece(true, 1, t + 1, s3, j & 1); }
  };
  unsigned curK = 0, curV = 0;
  auto setK = [&](unsigned kb0) __attribute__((always_inline)) {
    const unsigned d = kb0 - curK; curK = kb0;
#pragma unroll
    for (int i = 0; i < 8; ++i) { ak[i] += d; asm volatile("" : "+v"(ak[i])); }
  };
  auto setV = [&](unsigned vb0) __attribute__((always_inline)) {
    const unsigned d = vb0 - curV; curV = vb0;
#pragma unroll
    for (int i = 0; i < 4; ++i) { av[i] += d; asm volatile("" : "+v"(av[i])); }
  };
  constexpr int DL = LOOKAHEAD;
  bf16x8 kf[DL], vf[DL];
  auto rdK = [&](int f) __attribute__((always_inline)) { kf[f % DL] = lds_b128(ak[f & 7] + 8192u * (f >> 3)); };
  auto rdV = [&](int f) __attribute__((always_inline)) { vf[f % DL] = cat(lds_tr(av[f & 3] + 256u * (16u * (f >> 2))), lds_tr(av[f & 3] + 256u * (16u * (f >> 2) + 8u))); };
  auto QK = [&](int t, int j0, bool nextV) __attribute__((always_inline)) {
#pragma unroll
    for (int f = 0; f < 16; ++f) {
      SB();
      if ((f & 7) == 0) S[f >> 3] = MFMA32(kf[f % DL], qf[f & 7], negm);
      else S[f >> 3] = MFMA32(kf[f % DL], qf[f & 7], S[f >> 3]);
      SB();
      if (f + DL < 16) rdK(f + DL); else if (nextV) rdV(f + DL - 16);
      SB();
      if ((f & 3) == 1) issueB(t, j0 + (f >> 2));
    }
    SB();
  };
  auto SM = [&](bool first) __attribute__((always_inline)) {
    SB(); __builtin_amdgcn_s_setprio(2); SB();
    float pm0 = max3f(S[0][0], S[0][1], S[1][0]), pm1 = max3f(S[0][2], S[0][3], S[1][1]);
    pm0 = max3f(pm0, S[1][2], S[1][3]);
#pragma unroll
    for (int e = 4; e < 16; e += 4) {
      pm0 = max3f(pm0, S[0][e], S[0][e + 1]); pm1 = max3f(pm1, S[0][e + 2], S[0][e + 3]);
      pm0 = max3f(pm0, S[1][e], S[1][e + 1]); pm1 = max3f(pm1, S[1][e + 2], S[1][e + 3]);
    }
    float pm = max2f(pm0, pm1);
    { auto rr = __builtin_amdgcn_permlane32_swap(__float_as_uint(pm), __float_as_uint(pm), false, false); pm = max2f(__uint_as_float(rr[0]), __uint_as_float(rr[1])); }
    if (first || __builtin_expect(__any(!(pm <= THR)), 0)) {
      const float dl = first ? pm : max2f(pm, 0.f);
      m += dl;
      if (!first) {
        const float al = __builtin_amdgcn_exp2f(-dl);
        l *= al;
#pragma unroll
        for (int db = 0; db < 4; ++db)
#pragma unroll
          for (int e = 0; e < 16; ++e) o[db][e] *= al;
      }
#pragma unroll
      for (int e = 0; e < 16; ++e) { S[0][e] -= dl; S[1][e] -= dl; negm[e] = -m; }
    }
    float l0 = 0.f, l1 = 0.f;
#pragma unroll
    for (int kb = 0; kb < 2; ++kb) {
#pragma unroll
      for (int e = 0; e < 16; e += 2) {
        S[kb][e] = __builtin_amdgcn_exp2f(S[kb][e]); l0 += S[kb][e];
        S[kb][e + 1] = __builtin_amdgcn_exp2f(S[kb][e + 1]); l1 += S[kb][e + 1];
      }
#pragma unroll
      for (int u = 0; u < 2; ++u) {
        u32x4 w;
#pragma unroll
        for (int e = 0; e < 4; ++e) w[e] = cvtpk(S[kb][8 * u + 2 * e], S[kb][8 * u + 2 * e + 1]);
        pf[2 * kb + u] = __builtin_bit_cast(bf16x8, w);
      }
    }
    l += l0 + l1;
    asm volatile("" : "+v"(pf[0]), "+v"(pf[1]), "+v"(pf[2]), "+v"(pf[3]), "+v"(l));
    SB(); __builtin_amdgcn_s_setprio(0); SB();
  };
  auto waitB = [&](int t) __attribute__((always_inline)) { if (t == 8) WAITV(0); else if (t == 7) WAITV(2); else WAITV(4); BARRIER(); };
  if (hsel == 0) {
    unsigned sK = 9u, sV = 2u;
    auto stepA = [&](int t, auto firstc) __attribute__((always_inline)) {
      constexpr bool FIRST = decltype(firstc)::value;
      waitB(t);
      STAMP(11 + t);
      ISTAMP(47);
      slots();
      setK(L0 + sK * SLOT);
#pragma unroll
      for (int i = 0; i < 4; ++i) {
        SB();
#pragma unroll
        for (int f = i * DL / 4; f < (i + 1) * DL / 4; ++f) rdK(f);
        SB();
        if (!FIRST) o[i] = MFMA32(vf[(12 + i) % DL], pf[3], o[i]);
      }
      SB();
      setV(L0 + sV * SLOT);
      ISTAMP(43);
      QK(t, 0, true);
      ISTAMP(44);
      SM(FIRST);
      ISTAMP(45);
#pragma unroll
      for (int f = 0; f < 12; ++f) {
        SB();
        o[f & 3] = MFMA32(vf[f % DL], pf[f >> 2], o[f & 3]);
        SB();
        if (f + DL < 16) rdV(f + DL);
        SB();
        if ((f & 3) == 1) issueB(t, 4 + (f >> 2));
        if (f == 11) issueB(t, 7);
      }
      SB();
      ISTAMP(46);
      sK += 4u; if (sK >= 10u) sK -= 10u;
      sV += 4u; if (sV >= 10u) sV -= 10u;
    };
    stepA(0, std::true_type{});
#pragma unroll 1
    for (int t = 1; t < 8; ++t) stepA(t, std::false_type{});
    waitB(8);
    STAMP(19);
#pragma unroll
    for (int i = 0; i < 4; ++i) o[i] = MFMA32(vf[(12 + i) % DL], pf[3], o[i]);
  } else {
    unsigned sK = 1u, sV = 0u;
    auto stepB = [&](int t, auto firstc, auto lastc) __attribute__((always_inline)) {
      constexpr bool FIRST = decltype(firstc)::value, LAST = decltype(lastc)::value;
      waitB(t);
      STAMP(11 + t);
      if (!LAST) ISTAMP(47);
      slots();
      setV(L0 + sV * SLOT);
#pragma unroll
      for (int f = 0; f < DL; ++f) rdV(f);
      if (!LAST) setK(L0 + sK * SLOT);
      SB();
      if (!LAST) ISTAMP(43);
      if (!LAST) SM(FIRST);
      if (!LAST) ISTAMP(44);
#pragma unroll
      for (int f = 0; f < 16; ++f) {
        SB();
        o[f & 3] = MFMA32(vf[f % DL], pf[f >> 2], o[f & 3]);
        SB();
        if (f + DL < 16) rdV(f + DL); else if (!LAST) rdK(f + DL - 16);
        SB();
        if ((f & 3) == 1 && !LAST) issueB(t, f >> 2);
      }
      if (!LAST) ISTAMP(45);
      if (!LAST) QK(t, 4, false);
      SB();
      if (!LAST) ISTAMP(46);
      sK += 4u; if (sK >= 10u) sK -= 10u;
      sV += 4u; if (sV >= 10u) sV -= 10u;
    };
    {
      waitB(0);
      STAMP(11);
      slots();
      setK(L0 + sK * SLOT);
#pragma unroll
      for (int f = 0; f < DL; ++f) rdK(f);
#pragma unroll
      for (int j = 0; j < 4; ++j) issueB(0, j);
      QK(0, 4, false);
      sK = 5u; sV = 4u;
    }
    stepB(1, std::true_type{}, std::false_type{});
#pragma unroll 1
    for (int t = 2; t < 8; ++t) stepB(t, std::false_type{}, std::false_type{});
    SM(false);
    stepB(8, std::false_type{}, std::true_type{});
  }
  STAMP(27); CSTAMP(57);
  l = swapsum(l);
  const float inv = __builtin_amdgcn_rcpf(l);
  const rsrc_t rob = mkrsrc(Ows + (size_t)((br * NB + b) * NH + (h0 + hsel)) * 32768u, 32768u);
  const unsigned obo = (2u * qb + (l31 >> 4)) * 4096u + (l15 + 16u * hi) * 16u;
  auto ochunk = [&](unsigned c) __attribute__((always_inline)) { return L0 + (c < 3u ? 49152u + 8192u * c : 110592u + 8192u * (c - 3u)); };
  const unsigned olds = ochunk(wid) + (l31 >> 4) * 4096u + (l15 + 16u * hi) * 16u;
#pragma unroll
  for (int db = 0; db < 4; ++db)
#pragma unroll
    for (int u = 0; u < 2; ++u) {
      u32x4 w;
#pragma unroll
      for (int e = 0; e < 4; ++e) w[e] = cvtpk(o[db][8 * u + 2 * e] * inv, o[db][8 * u + 2 * e + 1] * inv);
      __builtin_amdgcn_raw_buffer_store_b128(w, rob, obo + db * 1024 + u * 512, 0, 16);
      *(LAS u32x4*)(size_t)(olds + db * 1024 + u * 512) = w;
    }
  STAMP(28); CSTAMP(61);

  {
  char* ws2 = Ows;
  const unsigned j = hp, kh = hsel;
  STAMP(32); CSTAMP(62);
  const rsrc_t rw = mkrsrc(br ? woB : woA, NH * DH * DM * 2);
  const rsrc_t ro = mkrsrc(ws2 + WS_O + (size_t)(br * NB + b) * NH * 32768u, NH * 32768u);
  unsigned vw[5];
#pragma unroll
  for (unsigned i = 0; i < 5; ++i) { const unsigned c = 64u * (qb + 4u * i) + lane; vw[i] = (c / 18u) * 1152u + (c % 18u) * 16u; }
#define ROT2J 2
  auto kpos = [&](int u) __attribute__((always_inline)) { return ((unsigned)u + 2u * j) & 7u; };
  const bool lowq = qb < 2u;
  const unsigned ldst = L0 + kh * 73728u + qb * 1024u;
  auto stage1 = [&](int u, int i) __attribute__((always_inline)) {
    if (i == 4 && !lowq) return;
    const unsigned so = ((hg * 8u + kpos(u)) * 128u + kh * 64u) * 1152u + j * 288u;
    dma16<W2_NT, 0>(ldst + (unsigned)(u & 3) * 18432u + (unsigned)i * 4096u, vw[i], rw, so);
  };
  const unsigned vo0 = lane * 16u + 2u * qb * 4096u, vo1 = vo0 + 4096u;
  bf16x8 bf[4][4];
  auto loadB = [&](int u, int f) __attribute__((always_inline)) {
    const unsigned so = (hg * 8u + kpos(u)) * 32768u + 2u * kh * 1024u;
    if (f == 0) asm volatile("buffer_load_dwordx4 %0, %1, %2, %3 offen sc1" : "=v"(bf[u & 3][0]) : "v"(vo0), "s"(ro), "s"(so) : "memory");
    if (f == 1) asm volatile("buffer_load_dwordx4 %0, %1, %2, %3 offen sc1" : "=v"(bf[u & 3][1]) : "v"(vo1), "s"(ro), "s"(so) : "memory");
    if (f == 2) asm volatile("buffer_load_dwordx4 %0, %1, %2, %3 offen offset:1024 sc1" : "=v"(bf[u & 3][2]) : "v"(vo0), "s"(ro), "s"(so) : "memory");
    if (f == 3) asm volatile("buffer_load_dwordx4 %0, %1, %2, %3 offen offset:1024 sc1" : "=v"(bf[u & 3][3]) : "v"(vo1), "s"(ro), "s"(so) : "memory");
  };
  auto issue = [&](int u, int v) __attribute__((always_inline)) {
    if (u > 7) return;
    if (v < 4) loadB(u, v); else if (v < 9) stage1(u, v - 4);
  };
  const unsigned aw = L0 + 288u * (16u * (lane >> 5) + 4u * ((lane >> 4) & 1u) + ((lane & 15u) >> 2)) + 8u * (lane & 3u) + kh * 73728u;
  f32x4 acc[9][2] = {};
  constexpr int DW = 6;
  bf16x8 fa[DW];
  auto rdW = [&](int F) __attribute__((always_inline)) {
    const int u = F / 18, e = (F % 18) / 9, nb = F % 9;
    const unsigned buf = aw + (unsigned)(u & 3) * 18432u + (unsigned)e * 9216u;
    fa[F % DW] = cat(lds_tr(buf + 32u * nb), lds_tr(buf + 288u * 8u + 32u * nb));
  };
  auto waitK2 = [&](int u, int N) __attribute__((always_inline)) {
    bf16x8* c = bf[u & 3];
    if (N == 0) WAITV(0);
    else { if (lowq) WAITV(9); else WAITV(8); }
    asm volatile("s_barrier" : "+v"(c[0]), "+v"(c[1]), "+v"(c[2]), "+v"(c[3]) :: "memory");
  };
  const unsigned long long tag = ((unsigned long long)hip_dispatch_id() + 1ull) * 0x9E3779B97F4A7C15ull ^ 0x6A09E667F3BCC908ull;
  GAS unsigned long long* fo = (GAS unsigned long long*)(ws2 + WS_FO + (size_t)((br * NB + b) * 4u + hg) * 16u * 128u);
#pragma unroll
  for (int i = 0; i < 5; ++i) stage1(0, i);
  BARRIER();
#pragma unroll
  for (int i = 0; i < 5; ++i) stage1(1, i);
#pragma unroll
  for (int u = 0; u < 2; ++u)
#pragma unroll
    for (int f = 0; f < 4; ++f) bf[u][f] = lds_b128(ochunk(4u * u + qb) + (unsigned)(f & 1) * 4096u + (2u * kh + (unsigned)(f >> 1)) * 1024u + lane * 16u);
  if (lowq) WAITV(5); else WAITV(4);
  BARRIER();
  if (wid == 0u && lane < 4u && lane != j) __hip_atomic_store(fo + (lane * 4u + j) * 16u, tag, __ATOMIC_RELAXED, __HIP_MEMORY_SCOPE_AGENT);
  STAMP(33);
  auto pollO = [&]() __attribute__((always_inline)) {
    if (wid == 0u) {
      const unsigned pl = lane & 3u;
      unsigned spins = 0;
      for (;;) {
        const unsigned long long v = pl == j ? tag : __hip_atomic_load(fo + (j * 4u + pl) * 16u, __ATOMIC_RELAXED, __HIP_MEMORY_SCOPE_AGENT);
        if (__all(v == tag) || ++spins > (1u << 18)) break;
        __builtin_amdgcn_s_sleep(1);
      }
      if (lane < 4u && lane != j) __hip_atomic_store(fo + (j * 4u + lane) * 16u, 0ull, __ATOMIC_RELAXED, __HIP_MEMORY_SCOPE_AGENT);
      WAITV(0);
    }
  };
#pragma unroll
  for (int F = 0; F < DW; ++F) rdW(F);
  sfor<144>([&](auto Fc) __attribute__((always_inline)) {
    constexpr int F = decltype(Fc)::value, u = F / 18, p = F % 18, e = p / 9, nb = p % 9;
    if constexpr (p == 18 - DW && u == 0) {
      SB(); pollO();
      if (lowq) WAITV(5); else WAITV(4);
      asm volatile("s_barrier" : "+v"(bf[1][0]), "+v"(bf[1][1]), "+v"(bf[1][2]), "+v"(bf[1][3]) :: "memory");
      STAMP(34); STAMP(52);
    }
    if constexpr (p == 18 - DW && u > 0 && u < 7) { SB(); waitK2(u + 1, u + 2 <= 7 ? 1 : 0); STAMP(34 + u); }
    SB();
    acc[nb][0] = MFMA16(fa[F % DW], bf[u & 3][2 * e], acc[nb][0]);
    acc[nb][1] = MFMA16(fa[F % DW], bf[u & 3][2 * e + 1], acc[nb][1]);
    SB();
    if constexpr (F + DW < 144) rdW(F + DW);
    SB();
    if constexpr (u == 0) { if constexpr (p >= 4 && p < 9) issue(2, p); if constexpr (p >= 18 - DW && p < 18 - DW + 4) issue(2, p - (18 - DW)); }
    else if constexpr (p < 9) issue(u + 2, p);
  });
  SB();
  BARRIER();
  STAMP(41);
  LAS f32x4* cb = (LAS f32x4*)(size_t)(L0 + wid * 9216u + lane * 16u);
  LAS f32x4* cr = (LAS f32x4*)(size_t)(L0 + (wid ^ 4u) * 9216u + lane * 16u);
#pragma unroll
  for (int nb = 0; nb < 9; ++nb) cb[nb * 64] = kh ? acc[nb][0] : acc[nb][1];
  BARRIER();
  f32x4 tot[9];
#pragma unroll
  for (int nb = 0; nb < 9; ++nb) tot[nb] = kh ? cr[nb * 64] + acc[nb][1] : acc[nb][0] + cr[nb * 64];
  STAMP(45);
  const unsigned grp = (br * NB + b) * 4u + j;
  const rsrc_t rp = mkrsrc(ws2 + WS_P + (size_t)grp * 16u * 18432u, 16u * 18432u);
  GAS unsigned long long* fp = (GAS unsigned long long*)(ws2 + WS_FP + (size_t)grp * 16u * 128u);
  LAS f32x4* own = (LAS f32x4*)(size_t)(L0 + 73728u + lane * 16u);
  if (qb != hg) {
#pragma unroll
    for (int nb = 0; nb < 9; ++nb)
      __builtin_amdgcn_raw_buffer_store_b128(__builtin_bit_cast(u32x4, tot[nb]), rp, lane * 16u + (unsigned)nb * 1024u, (qb * 4u + hg) * 18432u + kh * 9216u, 16);
    STAMP(46);
    WAITV(0);
    STAMP(47);
  } else {
#pragma unroll
    for (int nb = 0; nb < 9; ++nb) own[(kh * 9u + nb) * 64] = tot[nb];
  }
  BARRIER();
  if (wid == 0u) {
    if (lane < 4u && lane != hg) __hip_atomic_store(fp + (lane * 4u + hg) * 16u, tag, __ATOMIC_RELAXED, __HIP_MEMORY_SCOPE_AGENT);
    const unsigned pl = lane & 3u;
    unsigned spins = 0;
    for (;;) {
      const unsigned long long v = pl == hg ? tag : __hip_atomic_load(fp + (hg * 4u + pl) * 16u, __ATOMIC_RELAXED, __HIP_MEMORY_SCOPE_AGENT);
      if (__all(v == tag) || ++spins > (1u << 18)) break;
      __builtin_amdgcn_s_sleep(1);
    }
    if (lane < 4u && lane != hg) __hip_atomic_store(fp + (hg * 4u + lane) * 16u, 0ull, __ATOMIC_RELAXED, __HIP_MEMORY_SCOPE_AGENT);
  }
  STAMP(43);
  BARRIER();
  STAMP(44);
  {
    u32x4 pr[3][3];
    const bool third = wid < 2u;
#pragma unroll
    for (int i = 0; i < 3; ++i) {
      const unsigned tau = (i < 2 || third) ? wid + 8u * i : wid;
#pragma unroll
      for (unsigned s = 0; s < 3; ++s) { const unsigned src = s + (s >= hg ? 1u : 0u); pr[i][s] = __builtin_amdgcn_raw_buffer_load_b128(rp, lane * 16u + tau * 1024u, (hg * 4u + src) * 18432u, 16); }
    }
#pragma unroll
    for (int i = 0; i < 3; ++i) {
      if (i < 2 || third) {
        const unsigned tau = wid + 8u * i;
        const f32x4 v = own[tau * 64];
        const f32x4 p0 = __builtin_bit_cast(f32x4, pr[i][0]), p1 = __builtin_bit_cast(f32x4, pr[i][1]), p2 = __builtin_bit_cast(f32x4, pr[i][2]);
        const f32x4 t0 = hg == 0 ? v : p0, t1 = hg == 0 ? p0 : (hg == 1 ? v : p1), t2 = hg <= 1 ? p1 : (hg == 2 ? v : p2), t3 = hg == 3 ? v : p2;
        const f32x4 sum = (t0 + t1) + (t2 + t3);
        const unsigned qi = tau / 9u, nb = tau % 9u;
        const unsigned q = 32u * hg + 16u * qi + (lane & 15u), n = br * 576u + j * 144u + nb * 16u + 4u * (lane >> 4);
        u32x2 w = {cvtpk(sum[0], sum[1]), cvtpk(sum[2], sum[3])};
        __builtin_amdgcn_raw_buffer_store_b64(w, mkrsrc(out, 2359296u), ((b * SQ + q) * 1152u + n) * 2u, 0, 16);
      }
    }
  }
  STAMP(42); CSTAMP(63);
  }
}

extern "C" void kernel_launch(void* const* d_in, const int* in_sizes, int n_in, void* d_out, int out_size, void* d_ws, size_t ws_size, hipStream_t stream) {
  static bool attr = false;
  if (!attr) {
    (void)hipFuncSetAttribute((const void*)k_fused, hipFuncAttributeMaxDynamicSharedMemorySize, 163840);
    attr = true;
  }
  k_fused<<<256, 512, 163840, stream>>>((const char*)d_in[0], (const char*)d_in[5], (const char*)d_in[1], (const char*)d_in[6], (const char*)d_in[2], (const char*)d_in[7],
                                        (const char*)d_in[3], (const char*)d_in[8], (const char*)d_in[4], (const char*)d_in[9], (char*)d_ws + WS_O, (char*)d_out);
}
```

```cpp
#include <hip/hip_runtime.h>
#include <stdint.h>
#include <type_traits>
#include <utility>

#define LAS __attribute__((address_space(3)))
#define GAS __attribute__((address_space(1)))
#define DI __device__ __forceinline__
typedef __attribute__((ext_vector_type(8))) __bf16 bf16x8;
typedef __attribute__((ext_vector_type(4))) __bf16 bf16x4;
typedef __attribute__((ext_vector_type(2))) __bf16 bf16x2;
typedef __attribute__((ext_vector_type(16))) float f32x16;
typedef __attribute__((ext_vector_type(4))) float f32x4;
typedef __attribute__((ext_vector_type(2))) float f32x2;
typedef __attribute__((ext_vector_type(4))) unsigned u32x4;
typedef __attribute__((ext_vector_type(2))) unsigned u32x2;
typedef __amdgpu_buffer_rsrc_t rsrc_t;

constexpr int NB = 8, SQ = 128, DM = 576, NH = 32, SK = 512, DH = 128;
constexpr int SLOT = 16384;
constexpr size_t O_BYTES = (size_t)2 * NB * NH * SQ * DH * 2;
constexpr size_t WS_O = 0, WS_P = O_BYTES;
constexpr size_t P_BYTES = (size_t)64 * 4 * 4 * 18432;
constexpr size_t WS_FO = WS_P + P_BYTES, WS_FP = WS_FO + 1024 * 128;
constexpr float CSC = 0.08838834764831845f * 1.4426950408889634f;
constexpr float THR = 8.0f;

extern "C" __device__ unsigned long long hip_dispatch_id() __asm("llvm.amdgcn.dispatch.id");
DI unsigned cvtpk(float lo, float hi) { f32x2 v = {lo, hi}; bf16x2 b = __builtin_convertvector(v, bf16x2); return __builtin_bit_cast(unsigned, b); }
DI rsrc_t mkrsrc(const void* p, unsigned bytes) { return __builtin_amdgcn_make_buffer_rsrc((void*)p, (short)0, (int)bytes, 0x00020000); }
template <bool NT, int IMM> DI void dma16(unsigned ldsaddr, unsigned voff, rsrc_t r, unsigned soff) {
  if constexpr (NT) asm volatile("s_mov_b32 m0, %0\n\ts_nop 0\n\tbuffer_load_dwordx4 %1, %2, %3 offen offset:%c4 nt lds" ::"s"(ldsaddr), "v"(voff), "s"(r), "s"(soff), "i"(IMM) : "memory");
  else asm volatile("s_mov_b32 m0, %0\n\ts_nop 0\n\tbuffer_load_dwordx4 %1, %2, %3 offen offset:%c4 lds" ::"s"(ldsaddr), "v"(voff), "s"(r), "s"(soff), "i"(IMM) : "memory");
}
template <bool NT> DI void dma16i(int imm, unsigned ldsaddr, unsigned voff, rsrc_t r, unsigned soff) {
  switch (imm) {
    case 0: dma16<NT, 0>(ldsaddr, voff, r, soff); break;
    case 1: dma16<NT, 1024>(ldsaddr, voff, r, soff); break;
    case 2: dma16<NT, 2048>(ldsaddr, voff, r, soff); break;
    default: dma16<NT, 3072>(ldsaddr, voff, r, soff); break;
  }
}
template <int... I, class F> DI void sfor_impl(std::integer_sequence<int, I...>, F&& f) { (f(std::integral_constant<int, I>{}), ...); }
template <int N, class F> DI void sfor(F&& f) { sfor_impl(std::make_integer_sequence<int, N>{}, f); }
DI unsigned opq(unsigned x) { asm volatile("" : "+s"(x)); return x; }
#define WAITV(n) asm volatile("s_waitcnt vmcnt(" #n ")" ::: "memory")
#define BARRIER() asm volatile("s_waitcnt lgkmcnt(0)\n\ts_barrier" ::: "memory")
DI bf16x8 lds_b128(unsigned a) { return *(LAS bf16x8*)(size_t)a; }
DI bf16x4 lds_tr(unsigned a) { return __builtin_amdgcn_ds_read_tr16_b64_v4bf16((LAS bf16x4*)(size_t)a); }
DI bf16x8 cat(bf16x4 a, bf16x4 b) { return __builtin_shufflevector(a, b, 0, 1, 2, 3, 4, 5, 6, 7); }
DI float swapmax(float v) { auto r = __builtin_amdgcn_permlane32_swap(__float_as_uint(v), __float_as_uint(v), false, false); return fmaxf(__uint_as_float(r[0]), __uint_as_float(r[1])); }
DI float swapsum(float v) { auto r = __builtin_amdgcn_permlane32_swap(__float_as_uint(v), __float_as_uint(v), false, false); return __uint_as_float(r[0]) + __uint_as_float(r[1]); }
#define SB() __builtin_amdgcn_sched_barrier(0)
DI float max2f(float a, float b) { return __builtin_elementwise_maximum(a, b); }
DI float max3f(float a, float b, float c) { return max2f(max2f(a, b), c); }
#define MFMA32(a, b, c) __builtin_amdgcn_mfma_f32_32x32x16_bf16(a, b, c, 0, 0, 0)
#define MFMA16(a, b, c) __builtin_amdgcn_mfma_f32_16x16x32_bf16(a, b, c, 0, 0, 0)

#define STAMP(i) do {} while (0)
#define CSTAMP(i) do {} while (0)
#define STAMP_DECL do {} while (0)
#define ISTAMP(i) do {} while (0)
#define STAMP_FLUSH(base, lo, hi) do {} while (0)
#define KV_NT true
#define W_NT false
#define LOOKAHEAD 8
#define W2_NT false
#define TOUCHPOL ""
#define KVSTART 1

__global__ __launch_bounds__(512, 2) void k_fused(const char* __restrict__ xA, const char* __restrict__ xB, const char* __restrict__ kA, const char* __restrict__ kB,
                                                   const char* __restrict__ vA, const char* __restrict__ vB, const char* __restrict__ wqA, const char* __restrict__ wqB,
                                                   const char* __restrict__ woA, const char* __restrict__ woB, char* __restrict__ Ows, char* __restrict__ out) {
  extern __shared__ __attribute__((aligned(16))) char lds[];
  const unsigned tid = threadIdx.x, lane = tid & 63u;
  const unsigned wid = __builtin_amdgcn_readfirstlane(tid >> 6);
  STAMP_DECL;
  STAMP(0); CSTAMP(60);
  const unsigned bi = blockIdx.x, xcd = bi & 7u, r = bi >> 3, br = xcd >> 2, hg = xcd & 3u, b = r & 7u, hp = r >> 3, h0 = hg * 8u + hp * 2u;
  const unsigned L0 = (unsigned)(size_t)lds;
  const rsrc_t rx = mkrsrc((br ? xB : xA) + (size_t)b * SQ * DM * 2, SQ * DM * 2);
  const rsrc_t rw = mkrsrc(br ? wqB : wqA, DM * NH * DH * 2);
  const rsrc_t rk = mkrsrc((br ? kB : kA) + (size_t)(b * NH + h0) * SK * DH * 2, 2 * SK * DH * 2);
  const rsrc_t rv = mkrsrc((br ? vB : vA) + (size_t)(b * NH + h0) * SK * DH * 2, 2 * SK * DH * 2);
  const unsigned l15 = lane & 15u, l31 = lane & 31u, hi = lane >> 5, g4 = lane >> 4, blk = g4 & 1u, qq = l15 >> 2, pp = lane & 3u;
  const unsigned hsel = wid >> 2, qb = wid & 3u;

  unsigned vx[2], vw[2];
#pragma unroll
  for (unsigned j = 0; j < 2; ++j) {
    vx[j] = (lane >> 3) * 1152u + (((lane & 7u) ^ (4u * j + g4)) << 4) + j * (9216u - 1024u);
    vw[j] = g4 * 8192u + ((l15 ^ (g4 << 2)) << 4) + j * (32768u - 1024u);
  }
  const unsigned vv = g4 * 256u + ((l15 ^ (g4 << 2)) << 4);
  auto vk = [&](unsigned p) __attribute__((always_inline)) { return g4 * 256u + ((l15 ^ ((4u * (p & 3u) + g4) & 15u)) << 4); };
  const unsigned vkA = vk(2u * wid), vkB = vk(2u * wid + 1u);
  unsigned w2k = wid * 2048u;
  auto kv_piece = [&](bool isV, unsigned head, unsigned t, unsigned slot, int j) __attribute__((always_inline)) {
    const unsigned so = head * 131072u + t * 16384u + w2k, la = L0 + slot * SLOT + w2k;
    if (isV) dma16i<KV_NT>(j, la, vv, rv, so);
    else dma16i<KV_NT>(j, la, j ? vkB : vkA, rk, so);
  };

  const unsigned rot = b + 2u * hp + 7u;
  auto stage1 = [&](int i, int j) __attribute__((always_inline)) {
    const unsigned ks = i == 0 ? 0u : 1u + ((rot + (unsigned)i) & 7u);
    const unsigned buf = L0 + (unsigned)((i % 3) * 3) * SLOT + w2k;
    if (j < 2) dma16i<W_NT>(j, buf, vx[j], rx, wid * 18432u + ks * 128u);
    else { const unsigned h = (j >> 1) - 1u; dma16i<W_NT>(j & 1, buf + (1u + h) * SLOT, vw[j & 1], rw, (ks * 64u + 8u * wid) * 8192u + (h0 + h) * 256u); }
  };
  const rsrc_t rwo = mkrsrc((br ? woB : woA) + (size_t)hg * 1024u * 1152u, 1024u * 1152u);
  auto warmWo = [&](int i) __attribute__((always_inline)) {
    const unsigned dst = L0 + 9u * SLOT + (wid & 3u) * 4096u, so = r * 36864u + wid * 5120u;
    if (i < 4) dma16i<false>(i, dst, lane * 16u, rwo, so); else dma16<false, 0>(dst, lane * 16u, rwo, so + 4096u);
  };
  auto issueA = [&](int i, int j) __attribute__((always_inline)) {
    if (i == -1) { if (j >= 4 && j < 8) stage1(2, j - 4); return; }
    if (i <= 5) { if (j < 6) stage1(i + 3, j); }
  };
  unsigned ipf = 0;
  if (wid == 5u) {
    unsigned long long pc;
    asm volatile("s_getpc_b64 %0" : "=s"(pc));
    const char* cp = (const char*)(pc & ~0xFFFull) + (r & 7u) * 4608u + (lane < 36u ? lane : 35u) * 128u;
    asm volatile("global_load_dword %0, %1, off" : "=v"(ipf) : "v"(cp) : "memory");
  }
  STAMP(48);
#pragma unroll
  for (int j = 0; j < 6; ++j) stage1(0, j);
  STAMP(49);
  asm volatile("s_barrier" ::: "memory");
#pragma unroll
  for (int j = 0; j < 6; ++j) stage1(1, j);
  STAMP(50);
  unsigned ak[8], av[4];
#pragma unroll
  for (unsigned s = 0; s < 8; ++s) { ak[s] = 256u * l31 + (((2u * s + hi) ^ l15) << 4); asm volatile("" : "+v"(ak[s])); }
#pragma unroll
  for (unsigned db = 0; db < 4; ++db) { av[db] = 256u * (4u * hi + qq) + (((4u * db + 2u * blk + (pp >> 1)) ^ (qq << 2)) << 4) + 8u * (pp & 1u); asm volatile("" : "+v"(av[db])); }
  unsigned l2w = 0;
  f32x16 acc[4] = {};
  const unsigned xrow = 32u * qb + l31;
  unsigned ax[4], aw[4];
#pragma unroll
  for (unsigned ks = 0; ks < 4; ++ks) ax[ks] = L0 + xrow * 128u + (((2u * ks + hi) ^ ((xrow >> 1) & 7u)) << 4);
#pragma unroll
  for (unsigned db = 0; db < 4; ++db) aw[db] = L0 + 256u * (8u * hi + qq) + (((4u * db + 2u * blk + (pp & 1u)) ^ (qq << 2)) << 4) + 8u * (pp >> 1) + (1u + hsel) * SLOT;
  {
    bf16x8 fx[2], fa[2][4];
    auto rdA = [&](int i, int ks, int db) __attribute__((always_inline)) {
      const unsigned buf = (unsigned)((i % 3) * 3) * SLOT;
      if (db == 0) fx[ks & 1] = lds_b128(buf + ax[ks]);
      fa[ks & 1][db] = cat(lds_tr(buf + aw[db] + 256u * (16u * ks)), lds_tr(buf + aw[db] + 256u * (16u * ks + 4u)));
    };
    WAITV(6);
    asm volatile("" :: "v"(ipf));
    BARRIER();
    STAMP(1); CSTAMP(54);
#pragma unroll
    for (int db = 0; db < 4; ++db) rdA(0, 0, db);
#pragma unroll
    for (int i = 0; i < 9; ++i) {
#pragma unroll
      for (int ks = 0; ks < 4; ++ks) {
        if (ks == 3 && i < 8) {
          if (i == 1 || i == 2) WAITV(11); else if (i == 3) WAITV(7); else if (i == 4) WAITV(9); else if (i == 5) WAITV(8); else if (i == 7) WAITV(4); else WAITV(6);
          BARRIER();
          STAMP(2 + i); if (i == 7) CSTAMP(55);
        }
#pragma unroll
        for (int db = 0; db < 4; ++db) {
          SB();
          acc[db] = MFMA32(fa[ks & 1][db], fx[ks & 1], acc[db]);
          SB();
          if (ks < 3) rdA(i, ks + 1, db); else if (i < 8) rdA(i + 1, 0, db);
          SB();
          if (ks == 3) issueA(i, db); else if (ks == 0) issueA(i - 1, 4 + db);
          if (i == 0 && ks == 1 && db < 2) stage1(2, 4 + db);
          if (i == 1 && ks == 1) warmWo(db);
          if (i == 1 && ks == 2 && db == 0) warmWo(4);
          if (i == 3 && ks == 1 && db == 0) {
            const unsigned tl = wid >> 1, so = (tl >> 1) * 16384u + ((tl == 2u || tl == 3u) ? 131072u - 16384u : 0u) + 16384u + (wid & 1u) * 8192u;
            const unsigned so2 = (tl == 0u ? 16384u : tl == 1u ? 32768u : tl == 2u ? 131072u + 16384u : 131072u + 32768u) + (wid & 1u) * 8192u;
            (void)so;
            if ((tl & 1u) == 0u) asm volatile("buffer_load_dword %0, %1, %2, %3 offen" TOUCHPOL : "=v"(l2w) : "v"(lane * 128u), "s"(rv), "s"(so2) : "memory");
            else asm volatile("buffer_load_dword %0, %1, %2, %3 offen" TOUCHPOL : "=v"(l2w) : "v"(lane * 128u), "s"(rk), "s"(so2) : "memory");
          }
          if (i == 6 && ks == 0 && db == 0) asm volatile("" :: "v"(l2w));
          if (i == 4 && ks == 1 && db < 2) kv_piece(false, 0, 0, 9, db);
          if (i == 6 && ks == 3) { if (db < 2) kv_piece(false, 1, 0, 1, db); else kv_piece(true, 0, 0, 2, db - 2); }
          if (i == 7 && ks == 3) { if (db < 2) kv_piece(false, 0, 1, 3, db); else kv_piece(true, 1, 0, 4, db - 2); }
          if (i == 8 && ks == 0 && db < 2) kv_piece(false, 1, 1, 5, db);
        }
        SB();
      }
    }
  }
  bf16x8 qf[8];
#pragma unroll
  for (int s = 0; s < 8; ++s) {
    u32x4 w;
#pragma unroll
    for (int e = 0; e < 4; ++e) w[e] = cvtpk(acc[s >> 1][8 * (s & 1) + 2 * e] * CSC, acc[s >> 1][8 * (s & 1) + 2 * e + 1] * CSC);
    qf[s] = __builtin_bit_cast(bf16x8, w);
  }

  STAMP(10); CSTAMP(56);
  w2k = opq(wid * 2048u);
  f32x16 o[4];
  float m = 0.f, l = 0.f;
  f32x16 S[2], negm;
  bf16x8 pf[4];
  unsigned sn = 5u;
  auto nxt = [](unsigned s_) __attribute__((always_inline)) { return s_ == 9u ? 0u : s_ + 1u; };
  unsigned s0 = 0, s1 = 0, s2 = 0, s3 = 0;
  auto slots = [&]() __attribute__((always_inline)) { s0 = sn; s1 = nxt(s0); s2 = nxt(s1); s3 = nxt(s2); sn = nxt(s3); };
  auto issueB = [&](int t, int j) __attribute__((always_inline)) {
    const int q = j >> 1;
    if (q == 0) { if (t > 0 && t < 7) kv_piece(false, 1, t + 1, s0, j & 1); }
    else if (q == 1) { if (t < 7) kv_piece(true, 0, t + 1, s1, j & 1); }
    else if (q == 2) { if (t < 6) kv_piece(false, 0, t + 2, s2, j & 1); }
    else { if (t < 7) kv_piece(true, 1, t + 1, s3, j & 1); }
  };
  unsigned curK = 0, curV = 0;
  auto setK = [&](unsigned kb0) __attribute__((always_inline)) {
    const unsigned d = kb0 - curK; curK = kb0;
#pragma unroll
    for (int i = 0; i < 8; ++i) { ak[i] += d; asm volatile("" : "+v"(ak[i])); }
  };
  auto setV = [&](unsigned vb0) __attribute__((always_inline)) {
    const unsigned d = vb0 - curV; curV = vb0;
#pragma unroll
    for (int i = 0; i < 4; ++i) { av[i] += d; asm volatile("" : "+v"(av[i])); }
  };
  constexpr int DL = LOOKAHEAD;
  bf16x8 kf[DL], vf[DL];
  auto rdK = [&](int f) __attribute__((always_inline)) { kf[f % DL] = lds_b128(ak[f & 7] + 8192u * (f >> 3)); };
  auto rdV = [&](int f) __attribute__((always_inline)) { vf[f % DL] = cat(lds_tr(av[f & 3] + 256u * (16u * (f >> 2))), lds_tr(av[f & 3] + 256u * (16u * (f >> 2) + 8u))); };
  auto QK = [&](int t, int j0, bool nextV, bool first) __attribute__((always_inline)) {
#pragma unroll
    for (int f = 0; f < 16; ++f) {
      SB();
      if ((f & 7) == 0) { if (first) S[f >> 3] = MFMA32(kf[f % DL], qf[f & 7], f32x16{}); else S[f >> 3] = MFMA32(kf[f % DL], qf[f & 7], negm); }
      else S[f >> 3] = MFMA32(kf[f % DL], qf[f & 7], S[f >> 3]);
      SB();
      if (f + DL < 16) rdK(f + DL); else if (nextV) rdV(f + DL - 16);
      SB();
      if ((f & 3) == 1) issueB(t, j0 + (f >> 2));
    }
    SB();
  };
  auto SM = [&](bool first) __attribute__((always_inline)) {
    SB(); __builtin_amdgcn_s_setprio(2); SB();
    float pm0 = max3f(S[0][0], S[0][1], S[1][0]), pm1 = max3f(S[0][2], S[0][3], S[1][1]);
    pm0 = max3f(pm0, S[1][2], S[1][3]);
#pragma unroll
    for (int e = 4; e < 16; e += 4) {
      pm0 = max3f(pm0, S[0][e], S[0][e + 1]); pm1 = max3f(pm1, S[0][e + 2], S[0][e + 3]);
      pm0 = max3f(pm0, S[1][e], S[1][e + 1]); pm1 = max3f(pm1, S[1][e + 2], S[1][e + 3]);
    }
    float pm = max2f(pm0, pm1);
    { auto rr = __builtin_amdgcn_permlane32_swap(__float_as_uint(pm), __float_as_uint(pm), false, false); pm = max2f(__uint_as_float(rr[0]), __uint_as_float(rr[1])); }
    if (first || __builtin_expect(__any(!(pm <= THR)), 0)) {
      const float dl = first ? pm : max2f(pm, 0.f);
      m += dl;
      if (!first) {
        const float al = __builtin_amdgcn_exp2f(-dl);
        l *= al;
#pragma unroll
        for (int db = 0; db < 4; ++db)
#pragma unroll
          for (int e = 0; e < 16; ++e) o[db][e] *= al;
      }
#pragma unroll
      for (int e = 0; e < 16; ++e) { S[0][e] -= dl; S[1][e] -= dl; negm[e] = -m; }
    }
    float l0 = 0.f, l1 = 0.f;
#pragma unroll
    for (int kb = 0; kb < 2; ++kb) {
#pragma unroll
      for (int e = 0; e < 16; e += 2) {
        S[kb][e] = __builtin_amdgcn_exp2f(S[kb][e]); l0 += S[kb][e];
        S[kb][e + 1] = __builtin_amdgcn_exp2f(S[kb][e + 1]); l1 += S[kb][e + 1];
      }
#pragma unroll
      for (int u = 0; u < 2; ++u) {
        u32x4 w;
#pragma unroll
        for (int e = 0; e < 4; ++e) w[e] = cvtpk(S[kb][8 * u + 2 * e], S[kb][8 * u + 2 * e + 1]);
        pf[2 * kb + u] = __builtin_bit_cast(bf16x8, w);
      }
    }
    l += l0 + l1;
    asm volatile("" : "+v"(pf[0]), "+v"(pf[1]), "+v"(pf[2]), "+v"(pf[3]), "+v"(l));
    SB(); __builtin_amdgcn_s_setprio(0); SB();
  };
  auto waitB = [&](int t) __attribute__((always_inline)) { if (t == 8) WAITV(0); else if (t == 7) WAITV(2); else if (t == 0) WAITV(6); else WAITV(4); BARRIER(); };
  if (hsel == 0) {
    unsigned sK = 9u, sV = 2u;
    auto stepA = [&](int t, auto firstc) __attribute__((always_inline)) {
      constexpr bool FIRST = decltype(firstc)::value;
      waitB(t);
      STAMP(11 + t);
      ISTAMP(47);
      slots();
      setK(L0 + sK * SLOT);
#pragma unroll
      for (int i = 0; i < 4; ++i) {
        SB();
#pragma unroll
        for (int f = i * DL / 4; f < (i + 1) * DL / 4; ++f) rdK(f);
        SB();
        if (!FIRST) o[i] = MFMA32(vf[(12 + i) % DL], pf[3], o[i]);
      }
      SB();
      setV(L0 + sV * SLOT);
      ISTAMP(43);
      QK(t, 0, true, FIRST);
      ISTAMP(44);
      SM(FIRST);
      ISTAMP(45);
#pragma unroll
      for (int f = 0; f < 12; ++f) {
        SB();
        if (FIRST && f < 4) o[f & 3] = MFMA32(vf[f % DL], pf[f >> 2], f32x16{}); else o[f & 3] = MFMA32(vf[f % DL], pf[f >> 2], o[f & 3]);
        SB();
        if (f + DL < 16) rdV(f + DL);
        SB();
        if ((f & 3) == 1) issueB(t, 4 + (f >> 2));
        if (f == 11) issueB(t, 7);
      }
      SB();
      ISTAMP(46);
      sK += 4u; if (sK >= 10u) sK -= 10u;
      sV += 4u; if (sV >= 10u) sV -= 10u;
    };
    stepA(0, std::true_type{});
#pragma unroll 1
    for (int t = 1; t < 8; ++t) stepA(t, std::false_type{});
    waitB(8);
    STAMP(19);
#pragma unroll
    for (int i = 0; i < 4; ++i) o[i] = MFMA32(vf[(12 + i) % DL], pf[3], o[i]);
  } else {
    unsigned sK = 1u, sV = 0u;
    auto stepB = [&](int t, auto firstc, auto lastc) __attribute__((always_inline)) {
      constexpr bool FIRST = decltype(firstc)::value, LAST = decltype(lastc)::value;
      waitB(t);
      STAMP(11 + t);
      if (!LAST) ISTAMP(47);
      slots();
      setV(L0 + sV * SLOT);
#pragma unroll
      for (int f = 0; f < DL; ++f) rdV(f);
      if (!LAST) setK(L0 + sK * SLOT);
      SB();
      if (!LAST) ISTAMP(43);
      if (!LAST) SM(FIRST);
      if (!LAST) ISTAMP(44);
#pragma unroll
      for (int f = 0; f < 16; ++f) {
        SB();
        if (FIRST && f < 4) o[f & 3] = MFMA32(vf[f % DL], pf[f >> 2], f32x16{}); else o[f & 3] = MFMA32(vf[f % DL], pf[f >> 2], o[f & 3]);
        SB();
        if (f + DL < 16) rdV(f + DL); else if (!LAST) rdK(f + DL - 16);
        SB();
        if ((f & 3) == 1 && !LAST) issueB(t, f >> 2);
      }
      if (!LAST) ISTAMP(45);
      if (!LAST) QK(t, 4, false, false);
      SB();
      if (!LAST) ISTAMP(46);
      sK += 4u; if (sK >= 10u) sK -= 10u;
      sV += 4u; if (sV >= 10u) sV -= 10u;
    };
    {
      waitB(0);
      STAMP(11);
      slots();
      setK(L0 + sK * SLOT);
#pragma unroll
      for (int f = 0; f < DL; ++f) rdK(f);
#pragma unroll
      for (int j = 0; j < 4; ++j) issueB(0, j);
      QK(0, 4, false, true);
      sK = 5u; sV = 4u;
    }
    stepB(1, std::true_type{}, std::false_type{});
#pragma unroll 1
    for (int t = 2; t < 8; ++t) stepB(t, std::false_type{}, std::false_type{});
    SM(false);
    stepB(8, std::false_type{}, std::true_type{});
  }
  STAMP(27); CSTAMP(57);
  l = swapsum(l);
  const float inv = __builtin_amdgcn_rcpf(l);
  const rsrc_t rob = mkrsrc(Ows + (size_t)((br * NB + b) * NH + (h0 + hsel)) * 32768u, 32768u);
  const unsigned obo = (2u * qb + (l31 >> 4)) * 4096u + (l15 + 16u * hi) * 16u;
  auto ochunk = [&](unsigned c) __attribute__((always_inline)) { return L0 + (c < 3u ? 49152u + 8192u * c : 110592u + 8192u * (c - 3u)); };
  const unsigned olds = ochunk(wid) + (l31 >> 4) * 4096u + (l15 + 16u * hi) * 16u;
#pragma unroll
  for (int db = 0; db < 4; ++db)
#pragma unroll
    for (int u = 0; u < 2; ++u) {
      u32x4 w;
#pragma unroll
      for (int e = 0; e < 4; ++e) w[e] = cvtpk(o[db][8 * u + 2 * e] * inv, o[db][8 * u + 2 * e + 1] * inv);
      __builtin_amdgcn_raw_buffer_store_b128(w, rob, obo + db * 1024 + u * 512, 0, 16);
      *(LAS u32x4*)(size_t)(olds + db * 1024 + u * 512) = w;
    }
  STAMP(28); CSTAMP(61);

  {
  char* ws2 = Ows;
  const unsigned j = hp, kh = hsel;
  STAMP(32); CSTAMP(62);
  const rsrc_t rw = mkrsrc(br ? woB : woA, NH * DH * DM * 2);
  const rsrc_t ro = mkrsrc(ws2 + WS_O + (size_t)(br * NB + b) * NH * 32768u, NH * 32768u);
  unsigned vw[5];
#pragma unroll
  for (unsigned i = 0; i < 5; ++i) { const unsigned c = 64u * (qb + 4u * i) + lane; vw[i] = (c / 18u) * 1152u + (c % 18u) * 16u; }
#define ROT2J 2
  auto kpos = [&](int u) __attribute__((always_inline)) { return ((unsigned)u + 2u * j) & 7u; };
  const bool lowq = qb < 2u;
  const unsigned ldst = L0 + kh * 73728u + qb * 1024u;
  auto stage1 = [&](int u, int i) __attribute__((always_inline)) {
    if (i == 4 && !lowq) return;
    const unsigned so = ((hg * 8u + kpos(u)) * 128u + kh * 64u) * 1152u + j * 288u;
    dma16<W2_NT, 0>(ldst + (unsigned)(u & 3) * 18432u + (unsigned)i * 4096u, vw[i], rw, so);
  };
  const unsigned vo0 = lane * 16u + 2u * qb * 4096u, vo1 = vo0 + 4096u;
  bf16x8 bf[4][4];
  auto loadB = [&](int u, int f) __attribute__((always_inline)) {
    const unsigned so = (hg * 8u + kpos(u)) * 32768u + 2u * kh * 1024u;
    if (f == 0) asm volatile("buffer_load_dwordx4 %0, %1, %2, %3 offen sc1" : "=v"(bf[u & 3][0]) : "v"(vo0), "s"(ro), "s"(so) : "memory");
    if (f == 1) asm volatile("buffer_load_dwordx4 %0, %1, %2, %3 offen sc1" : "=v"(bf[u & 3][1]) : "v"(vo1), "s"(ro), "s"(so) : "memory");
    if (f == 2) asm volatile("buffer_load_dwordx4 %0, %1, %2, %3 offen offset:1024 sc1" : "=v"(bf[u & 3][2]) : "v"(vo0), "s"(ro), "s"(so) : "memory");
    if (f == 3) asm volatile("buffer_load_dwordx4 %0, %1, %2, %3 offen offset:1024 sc1" : "=v"(bf[u & 3][3]) : "v"(vo1), "s"(ro), "s"(so) : "memory");
  };
  auto issue = [&](int u, int v) __attribute__((always_inline)) {
    if (u > 7) return;
    if (v < 4) loadB(u, v); else if (v < 9) stage1(u, v - 4);
  };
  const unsigned aw = L0 + 288u * (16u * (lane >> 5) + 4u * ((lane >> 4) & 1u) + ((lane & 15u) >> 2)) + 8u * (lane & 3u) + kh * 73728u;
  f32x4 acc[9][2] = {};
  constexpr int DW = 6;
  bf16x8 fa[DW];
  auto rdW = [&](int F) __attribute__((always_inline)) {
    const int u = F / 18, e = (F % 18) / 9, nb = F % 9;
    const unsigned buf = aw + (unsigned)(u & 3) * 18432u + (unsigned)e * 9216u;
    fa[F % DW] = cat(lds_tr(buf + 32u * nb), lds_tr(buf + 288u * 8u + 32u * nb));
  };
  auto waitK2 = [&](int u, int N) __attribute__((always_inline)) {
    bf16x8* c = bf[u & 3];
    if (N == 0) WAITV(0);
    else { if (lowq) WAITV(9); else WAITV(8); }
    asm volatile("s_barrier" : "+v"(c[0]), "+v"(c[1]), "+v"(c[2]), "+v"(c[3]) :: "memory");
  };
  const unsigned long long tag = ((unsigned long long)hip_dispatch_id() + 1ull) * 0x9E3779B97F4A7C15ull ^ 0x6A09E667F3BCC908ull;
  GAS unsigned long long* fo = (GAS unsigned long long*)(ws2 + WS_FO + (size_t)((br * NB + b) * 4u + hg) * 16u * 128u);
#pragma unroll
  for (int i = 0; i < 5; ++i) stage1(0, i);
  BARRIER();
#pragma unroll
  for (int i = 0; i < 5; ++i) stage1(1, i);
#pragma unroll
  for (int u = 0; u < 2; ++u)
#pragma unroll
    for (int f = 0; f < 4; ++f) bf[u][f] = lds_b128(ochunk(4u * u + qb) + (unsigned)(f & 1) * 4096u + (2u * kh + (unsigned)(f >> 1)) * 1024u + lane * 16u);
  if (lowq) WAITV(5); else WAITV(4);
  BARRIER();
  if (wid == 0u && lane < 4u && lane != j) __hip_atomic_store(fo + (lane * 4u + j) * 16u, tag, __ATOMIC_RELAXED, __HIP_MEMORY_SCOPE_AGENT);
  STAMP(33);
  auto pollO = [&]() __attribute__((always_inline)) {
    if (wid == 0u) {
      const unsigned pl = lane & 3u;
      unsigned spins = 0;
      for (;;) {
        const unsigned long long v = pl == j ? tag : __hip_atomic_load(fo + (j * 4u + pl) * 16u, __ATOMIC_RELAXED, __HIP_MEMORY_SCOPE_AGENT);
        if (__all(v == tag) || ++spins > (1u << 18)) break;
        __builtin_amdgcn_s_sleep(1);
      }
      if (lane < 4u && lane != j) __hip_atomic_store(fo + (j * 4u + lane) * 16u, 0ull, __ATOMIC_RELAXED, __HIP_MEMORY_SCOPE_AGENT);
      WAITV(0);
    }
  };
#pragma unroll
  for (int F = 0; F < DW; ++F) rdW(F);
  sfor<144>([&](auto Fc) __attribute__((always_inline)) {
    constexpr int F = decltype(Fc)::value, u = F / 18, p = F % 18, e = p / 9, nb = p % 9;
    if constexpr (p == 18 - DW && u == 0) {
      SB(); pollO();
      if (lowq) WAITV(5); else WAITV(4);
      asm volatile("s_barrier" : "+v"(bf[1][0]), "+v"(bf[1][1]), "+v"(bf[1][2]), "+v"(bf[1][3]) :: "memory");
      STAMP(34); STAMP(52);
    }
    if constexpr (p == 18 - DW && u > 0 && u < 7) { SB(); waitK2(u + 1, u + 2 <= 7 ? 1 : 0); STAMP(34 + u); }
    SB();
    acc[nb][0] = MFMA16(fa[F % DW], bf[u & 3][2 * e], acc[nb][0]);
    acc[nb][1] = MFMA16(fa[F % DW], bf[u & 3][2 * e + 1], acc[nb][1]);
    SB();
    if constexpr (F + DW < 144) rdW(F + DW);
    SB();
    if constexpr (u == 0) { if constexpr (p >= 4 && p < 9) issue(2, p); if constexpr (p >= 18 - DW && p < 18 - DW + 4) issue(2, p - (18 - DW)); }
    else if constexpr (p < 9) issue(u + 2, p);
  });
  SB();
  BARRIER();
  STAMP(41);
  LAS f32x4* cb = (LAS f32x4*)(size_t)(L0 + wid * 9216u + lane * 16u);
  LAS f32x4* cr = (LAS f32x4*)(size_t)(L0 + (wid ^ 4u) * 9216u + lane * 16u);
#pragma unroll
  for (int nb = 0; nb < 9; ++nb) cb[nb * 64] = kh ? acc[nb][0] : acc[nb][1];
  BARRIER();
  f32x4 tot[9];
#pragma unroll
  for (int nb = 0; nb < 9; ++nb) tot[nb] = kh ? cr[nb * 64] + acc[nb][1] : acc[nb][0] + cr[nb * 64];
  STAMP(45);
  const unsigned grp = (br * NB + b) * 4u + j;
  const rsrc_t rp = mkrsrc(ws2 + WS_P + (size_t)grp * 16u * 18432u, 16u * 18432u);
  GAS unsigned long long* fp = (GAS unsigned long long*)(ws2 + WS_FP + (size_t)grp * 16u * 128u);
  LAS f32x4* own = (LAS f32x4*)(size_t)(L0 + 73728u + lane * 16u);
  if (qb != hg) {
#pragma unroll
    for (int nb = 0; nb < 9; ++nb)
      __builtin_amdgcn_raw_buffer_store_b128(__builtin_bit_cast(u32x4, tot[nb]), rp, lane * 16u + (unsigned)nb * 1024u, (qb * 4u + hg) * 18432u + kh * 9216u, 16);
    STAMP(46);
    WAITV(0);
    STAMP(47);
  } else {
#pragma unroll
    for (int nb = 0; nb < 9; ++nb) own[(kh * 9u + nb) * 64] = tot[nb];
  }
  BARRIER();
  if (wid == 0u) {
    if (lane < 4u && lane != hg) __hip_atomic_store(fp + (lane * 4u + hg) * 16u, tag, __ATOMIC_RELAXED, __HIP_MEMORY_SCOPE_AGENT);
    const unsigned pl = lane & 3u;
    unsigned spins = 0;
    for (;;) {
      const unsigned long long v = pl == hg ? tag : __hip_atomic_load(fp + (hg * 4u + pl) * 16u, __ATOMIC_RELAXED, __HIP_MEMORY_SCOPE_AGENT);
      if (__all(v == tag) || ++spins > (1u << 18)) break;
      __builtin_amdgcn_s_sleep(1);
    }
    if (lane < 4u && lane != hg) __hip_atomic_store(fp + (hg * 4u + lane) * 16u, 0ull, __ATOMIC_RELAXED, __HIP_MEMORY_SCOPE_AGENT);
  }
  STAMP(43);
  BARRIER();
  STAMP(44);
  {
    u32x4 pr[3][3];
    const bool third = wid < 2u;
#pragma unroll
    for (int i = 0; i < 3; ++i) {
      const unsigned tau = (i < 2 || third) ? wid + 8u * i : wid;
#pragma unroll
      for (unsigned s = 0; s < 3; ++s) { const unsigned src = s + (s >= hg ? 1u : 0u); pr[i][s] = __builtin_amdgcn_raw_buffer_load_b128(rp, lane * 16u + tau * 1024u, (hg * 4u + src) * 18432u, 16); }
    }
#pragma unroll
    for (int i = 0; i < 3; ++i) {
      if (i < 2 || third) {
        const unsigned tau = wid + 8u * i;
        const f32x4 v = own[tau * 64];
        const f32x4 p0 = __builtin_bit_cast(f32x4, pr[i][0]), p1 = __builtin_bit_cast(f32x4, pr[i][1]), p2 = __builtin_bit_cast(f32x4, pr[i][2]);
        const f32x4 t0 = hg == 0 ? v : p0, t1 = hg == 0 ? p0 : (hg == 1 ? v : p1), t2 = hg <= 1 ? p1 : (hg == 2 ? v : p2), t3 = hg == 3 ? v : p2;
        const f32x4 sum = (t0 + t1) + (t2 + t3);
        const unsigned qi = tau / 9u, nb = tau % 9u;
        const unsigned q = 32u * hg + 16u * qi + (lane & 15u), n = br * 576u + j * 144u + nb * 16u + 4u * (lane >> 4);
        u32x2 w = {cvtpk(sum[0], sum[1]), cvtpk(sum[2], sum[3])};
        *(GAS u32x2*)(out + ((size_t)(b * SQ + q) * 1152u + n) * 2u) = w;
      }
    }
  }
  STAMP(42); CSTAMP(63);
  }
}

extern "C" void kernel_launch(void* const* d_in, const int* in_sizes, int n_in, void* d_out, int out_size, void* d_ws, size_t ws_size, hipStream_t stream) {
  static bool attr = false;
  if (!attr) {
    (void)hipFuncSetAttribute((const void*)k_fused, hipFuncAttributeMaxDynamicSharedMemorySize, 163840);
    attr = true;
  }
  k_fused<<<256, 512, 163840, stream>>>((const char*)d_in[0], (const char*)d_in[5], (const char*)d_in[1], (const char*)d_in[6], (const char*)d_in[2], (const char*)d_in[7],
                                        (const char*)d_in[3], (const char*)d_in[8], (const char*)d_in[4], (const char*)d_in[9], (char*)d_ws + WS_O, (char*)d_out);
}
```

```cpp
#include <hip/hip_runtime.h>
#include <stdint.h>
#include <type_traits>
#include <utility>

#define LAS __attribute__((address_space(3)))
#define GAS __attribute__((address_space(1)))
#define DI __device__ __forceinline__
typedef __attribute__((ext_vector_type(8))) __bf16 bf16x8;
typedef __attribute__((ext_vector_type(4))) __bf16 bf16x4;
typedef __attribute__((ext_vector_type(2))) __bf16 bf16x2;
typedef __attribute__((ext_vector_type(16))) float f32x16;
typedef __attribute__((ext_vector_type(4))) float f32x4;
typedef __attribute__((ext_vector_type(2))) float f32x2;
typedef __attribute__((ext_vector_type(4))) unsigned u32x4;
typedef __attribute__((ext_vector_type(2))) unsigned u32x2;
typedef __amdgpu_buffer_rsrc_t rsrc_t;

constexpr int NB = 8, SQ = 128, DM = 576, NH = 32, SK = 512, DH = 128;
constexpr int SLOT = 16384;
constexpr size_t O_BYTES = (size_t)2 * NB * NH * SQ * DH * 2;
constexpr size_t WS_O = 0, WS_P = O_BYTES;
constexpr size_t P_BYTES = (size_t)64 * 4 * 4 * 18432;
constexpr size_t WS_FO = WS_P + P_BYTES, WS_FP = WS_FO + 1024 * 128;
constexpr float CSC = 0.08838834764831845f * 1.4426950408889634f;
constexpr float THR = 8.0f;

extern "C" __device__ unsigned long long hip_dispatch_id() __asm("llvm.amdgcn.dispatch.id");
DI unsigned cvtpk(float lo, float hi) { f32x2 v = {lo, hi}; bf16x2 b = __builtin_convertvector(v, bf16x2); return __builtin_bit_cast(unsigned, b); }
DI rsrc_t mkrsrc(const void* p, unsigned bytes) { return __builtin_amdgcn_make_buffer_rsrc((void*)p, (short)0, (int)bytes, 0x00020000); }
template <bool NT, int IMM> DI void dma16(unsigned ldsaddr, unsigned voff, rsrc_t r, unsigned soff) {
  if constexpr (NT) asm volatile("s_mov_b32 m0, %0\n\ts_nop 0\n\tbuffer_load_dwordx4 %1, %2, %3 offen offset:%c4 nt lds" ::"s"(ldsaddr), "v"(voff), "s"(r), "s"(soff), "i"(IMM) : "memory");
  else asm volatile("s_mov_b32 m0, %0\n\ts_nop 0\n\tbuffer_load_dwordx4 %1, %2, %3 offen offset:%c4 lds" ::"s"(ldsaddr), "v"(voff), "s"(r), "s"(soff), "i"(IMM) : "memory");
}
template <bool NT> DI void dma16i(int imm, unsigned ldsaddr, unsigned voff, rsrc_t r, unsigned soff) {
  switch (imm) {
    case 0: dma16<NT, 0>(ldsaddr, voff, r, soff); break;
    case 1: dma16<NT, 1024>(ldsaddr, voff, r, soff); break;
    case 2: dma16<NT, 2048>(ldsaddr, voff, r, soff); break;
    default: dma16<NT, 3072>(ldsaddr, voff, r, soff); break;
  }
}
template <int... I, class F> DI void sfor_impl(std::integer_sequence<int, I...>, F&& f) { (f(std::integral_constant<int, I>{}), ...); }
template <int N, class F> DI void sfor(F&& f) { sfor_impl(std::make_integer_sequence<int, N>{}, f); }
DI unsigned opq(unsigned x) { asm volatile("" : "+s"(x)); return x; }
#define WAITV(n) asm volatile("s_waitcnt vmcnt(" #n ")" ::: "memory")
#define BARRIER() asm volatile("s_waitcnt lgkmcnt(0)\n\ts_barrier" ::: "memory")
DI bf16x8 lds_b128(unsigned a) { return *(LAS bf16x8*)(size_t)a; }
DI bf16x4 lds_tr(unsigned a) { return __builtin_amdgcn_ds_read_tr16_b64_v4bf16((LAS bf16x4*)(size_t)a); }
DI bf16x8 cat(bf16x4 a, bf16x4 b) { return __builtin_shufflevector(a, b, 0, 1, 2, 3, 4, 5, 6, 7); }
DI float swapsum(float v) { auto r = __builtin_amdgcn_permlane32_swap(__float_as_uint(v), __float_as_uint(v), false, false); return __uint_as_float(r[0]) + __uint_as_float(r[1]); }
#define SB() __builtin_amdgcn_sched_barrier(0)
DI float max2f(float a, float b) { return __builtin_elementwise_maximum(a, b); }
DI float max3f(float a, float b, float c) { return max2f(max2f(a, b), c); }
#define MFMA32(a, b, c) __builtin_amdgcn_mfma_f32_32x32x16_bf16(a, b, c, 0, 0, 0)
#define MFMA16(a, b, c) __builtin_amdgcn_mfma_f32_16x16x32_bf16(a, b, c, 0, 0, 0)

#define KV_NT true
#define W_NT false
#define LOOKAHEAD 8
#define W2_NT false

__global__ __launch_bounds__(512, 2) void k_fused(const char* __restrict__ xA, const char* __restrict__ xB, const char* __restrict__ kA, const char* __restrict__ kB,
                                                   const char* __restrict__ vA, const char* __restrict__ vB, const char* __restrict__ wqA, const char* __restrict__ wqB,
                                                   const char* __restrict__ woA, const char* __restrict__ woB, char* __restrict__ Ows, char* __restrict__ out) {
  extern __shared__ __attribute__((aligned(16))) char lds[];
  const unsigned tid = threadIdx.x, lane = tid & 63u;
  const unsigned wid = __builtin_amdgcn_readfirstlane(tid >> 6);

  const unsigned bi = blockIdx.x, xcd = bi & 7u, r = bi >> 3, br = xcd >> 2, hg = xcd & 3u, b = r & 7u, hp = r >> 3, h0 = hg * 8u + hp * 2u;
  const unsigned L0 = (unsigned)(size_t)lds;
  const rsrc_t rx = mkrsrc((br ? xB : xA) + (size_t)b * SQ * DM * 2, SQ * DM * 2);
  const rsrc_t rw = mkrsrc(br ? wqB : wqA, DM * NH * DH * 2);
  const rsrc_t rk = mkrsrc((br ? kB : kA) + (size_t)(b * NH + h0) * SK * DH * 2, 2 * SK * DH * 2);
  const rsrc_t rv = mkrsrc((br ? vB : vA) + (size_t)(b * NH + h0) * SK * DH * 2, 2 * SK * DH * 2);
  const unsigned l15 = lane & 15u, l31 = lane & 31u, hi = lane >> 5, g4 = lane >> 4, blk = g4 & 1u, qq = l15 >> 2, pp = lane & 3u;
  const unsigned hsel = wid >> 2, qb = wid & 3u;

  unsigned vx[2], vw[2];
#pragma unroll
  for (unsigned j = 0; j < 2; ++j) {
    vx[j] = (lane >> 3) * 1152u + (((lane & 7u) ^ (4u * j + g4)) << 4) + j * (9216u - 1024u);
    vw[j] = g4 * 8192u + ((l15 ^ (g4 << 2)) << 4) + j * (32768u - 1024u);
  }
  const unsigned vv = g4 * 256u + ((l15 ^ (g4 << 2)) << 4);
  auto vk = [&](unsigned p) __attribute__((always_inline)) { return g4 * 256u + ((l15 ^ ((4u * (p & 3u) + g4) & 15u)) << 4); };
  const unsigned vkA = vk(2u * wid), vkB = vk(2u * wid + 1u);
  unsigned w2k = wid * 2048u;
  auto kv_piece = [&](bool isV, unsigned head, unsigned t, unsigned slot, int j) __attribute__((always_inline)) {
    const unsigned so = head * 131072u + t * 16384u + w2k, la = L0 + slot * SLOT + w2k;
    if (isV) dma16i<KV_NT>(j, la, vv, rv, so);
    else dma16i<KV_NT>(j, la, j ? vkB : vkA, rk, so);
  };

  const unsigned rot = b + 2u * hp + 7u;
  auto stage1 = [&](int i, int j) __attribute__((always_inline)) {
    const unsigned ks = i == 0 ? 0u : 1u + ((rot + (unsigned)i) & 7u);
    const unsigned buf = L0 + (unsigned)((i % 3) * 3) * SLOT + w2k;
    if (j < 2) dma16i<W_NT>(j, buf, vx[j], rx, wid * 18432u + ks * 128u);
    else { const unsigned h = (j >> 1) - 1u; dma16i<W_NT>(j & 1, buf + (1u + h) * SLOT, vw[j & 1], rw, (ks * 64u + 8u * wid) * 8192u + (h0 + h) * 256u); }
  };
  const rsrc_t rwo = mkrsrc((br ? woB : woA) + (size_t)hg * 1024u * 1152u, 1024u * 1152u);
  auto warmWo = [&](int i) __attribute__((always_inline)) {
    const unsigned dst = L0 + 9u * SLOT + (wid & 3u) * 4096u, so = r * 36864u + wid * 5120u;
    if (i < 4) dma16i<false>(i, dst, lane * 16u, rwo, so); else dma16<false, 0>(dst, lane * 16u, rwo, so + 4096u);
  };
  auto issueA = [&](int i, int j) __attribute__((always_inline)) {
    if (i == -1) { if (j >= 4 && j < 8) stage1(2, j - 4); return; }
    if (i <= 5) { if (j < 6) stage1(i + 3, j); }
  };
#pragma unroll
  for (int j = 0; j < 6; ++j) stage1(0, j);

  asm volatile("s_barrier" ::: "memory");
#pragma unroll
  for (int j = 0; j < 6; ++j) stage1(1, j);

  unsigned ak[8], av[4];
#pragma unroll
  for (unsigned s = 0; s < 8; ++s) { ak[s] = 256u * l31 + (((2u * s + hi) ^ l15) << 4); asm volatile("" : "+v"(ak[s])); }
#pragma unroll
  for (unsigned db = 0; db < 4; ++db) { av[db] = 256u * (4u * hi + qq) + (((4u * db + 2u * blk + (pp >> 1)) ^ (qq << 2)) << 4) + 8u * (pp & 1u); asm volatile("" : "+v"(av[db])); }
  f32x16 acc[4] = {};
  const unsigned xrow = 32u * qb + l31;
  unsigned ax[4], aw[4];
#pragma unroll
  for (unsigned ks = 0; ks < 4; ++ks) ax[ks] = L0 + xrow * 128u + (((2u * ks + hi) ^ ((xrow >> 1) & 7u)) << 4);
#pragma unroll
  for (unsigned db = 0; db < 4; ++db) aw[db] = L0 + 256u * (8u * hi + qq) + (((4u * db + 2u * blk + (pp & 1u)) ^ (qq << 2)) << 4) + 8u * (pp >> 1) + (1u + hsel) * SLOT;
  {
    bf16x8 fx[2], fa[2][4];
    auto rdA = [&](int i, int ks, int db) __attribute__((always_inline)) {
      const unsigned buf = (unsigned)((i % 3) * 3) * SLOT;
      if (db == 0) fx[ks & 1] = lds_b128(buf + ax[ks]);
      fa[ks & 1][db] = cat(lds_tr(buf + aw[db] + 256u * (16u * ks)), lds_tr(buf + aw[db] + 256u * (16u * ks + 4u)));
    };
    WAITV(6);
    BARRIER();

#pragma unroll
    for (int db = 0; db < 4; ++db) rdA(0, 0, db);
#pragma unroll
    for (int i = 0; i < 9; ++i) {
#pragma unroll
      for (int ks = 0; ks < 4; ++ks) {
        if (ks == 3 && i < 8) {
          if (i == 1 || i == 2) WAITV(11); else if (i == 4 || i == 5) WAITV(8); else if (i == 7) WAITV(4); else WAITV(6);
          BARRIER();

        }
#pragma unroll
        for (int db = 0; db < 4; ++db) {
          SB();
          acc[db] = MFMA32(fa[ks & 1][db], fx[ks & 1], acc[db]);
          SB();
          if (ks < 3) rdA(i, ks + 1, db); else if (i < 8) rdA(i + 1, 0, db);
          SB();
          if (ks == 3) issueA(i, db); else if (ks == 0) issueA(i - 1, 4 + db);
          if (i == 0 && ks == 1 && db < 2) stage1(2, 4 + db);
          if (i == 1 && ks == 1) warmWo(db);
          if (i == 1 && ks == 2 && db == 0) warmWo(4);
          if (i == 4 && ks == 1 && db < 2) kv_piece(false, 0, 0, 9, db);
          if (i == 6 && ks == 3) { if (db < 2) kv_piece(false, 1, 0, 1, db); else kv_piece(true, 0, 0, 2, db - 2); }
          if (i == 7 && ks == 3) { if (db < 2) kv_piece(false, 0, 1, 3, db); else kv_piece(true, 1, 0, 4, db - 2); }
          if (i == 8 && ks == 0 && db < 2) kv_piece(false, 1, 1, 5, db);
        }
        SB();
      }
    }
  }
  bf16x8 qf[8];
#pragma unroll
  for (int s = 0; s < 8; ++s) {
    u32x4 w;
#pragma unroll
    for (int e = 0; e < 4; ++e) w[e] = cvtpk(acc[s >> 1][8 * (s & 1) + 2 * e] * CSC, acc[s >> 1][8 * (s & 1) + 2 * e + 1] * CSC);
    qf[s] = __builtin_bit_cast(bf16x8, w);
  }

  w2k = opq(wid * 2048u);
  f32x16 o[4];
  float m = 0.f, l = 0.f;
  f32x16 S[2], negm;
  bf16x8 pf[4];
  unsigned sn = 5u;
  auto nxt = [](unsigned s_) __attribute__((always_inline)) { return s_ == 9u ? 0u : s_ + 1u; };
  unsigned s0 = 0, s1 = 0, s2 = 0, s3 = 0;
  auto slots = [&]() __attribute__((always_inline)) { s0 = sn; s1 = nxt(s0); s2 = nxt(s1); s3 = nxt(s2); sn = nxt(s3); };
  auto issueB = [&](int t, int j) __attribute__((always_inline)) {
    const int q = j >> 1;
    if (q == 0) { if (t > 0 && t < 7) kv_piece(false, 1, t + 1, s0, j & 1); }
    else if (q == 1) { if (t < 7) kv_piece(true, 0, t + 1, s1, j & 1); }
    else if (q == 2) { if (t < 6) kv_piece(false, 0, t + 2, s2, j & 1); }
    else { if (t < 7) kv_piece(true, 1, t + 1, s3, j & 1); }
  };
  unsigned curK = 0, curV = 0;
  auto setK = [&](unsigned kb0) __attribute__((always_inline)) {
    const unsigned d = kb0 - curK; curK = kb0;
#pragma unroll
    for (int i = 0; i < 8; ++i) { ak[i] += d; asm volatile("" : "+v"(ak[i])); }
  };
  auto setV = [&](unsigned vb0) __attribute__((always_inline)) {
    const unsigned d = vb0 - curV; curV = vb0;
#pragma unroll
    for (int i = 0; i < 4; ++i) { av[i] += d; asm volatile("" : "+v"(av[i])); }
  };
  constexpr int DL = LOOKAHEAD;
  bf16x8 kf[DL], vf[DL];
  auto rdK = [&](int f) __attribute__((always_inline)) { kf[f % DL] = lds_b128(ak[f & 7] + 8192u * (f >> 3)); };
  auto rdV = [&](int f) __attribute__((always_inline)) { vf[f % DL] = cat(lds_tr(av[f & 3] + 256u * (16u * (f >> 2))), lds_tr(av[f & 3] + 256u * (16u * (f >> 2) + 8u))); };
  auto QK = [&](int t, int j0, bool nextV, bool first) __attribute__((always_inline)) {
#pragma unroll
    for (int f = 0; f < 16; ++f) {
      SB();
      if ((f & 7) == 0) { if (first) S[f >> 3] = MFMA32(kf[f % DL], qf[f & 7], f32x16{}); else S[f >> 3] = MFMA32(kf[f % DL], qf[f & 7], negm); }
      else S[f >> 3] = MFMA32(kf[f % DL], qf[f & 7], S[f >> 3]);
      SB();
      if (f + DL < 16) rdK(f + DL); else if (nextV) rdV(f + DL - 16);
      SB();
      if ((f & 3) == 1) issueB(t, j0 + (f >> 2));
    }
    SB();
  };
  auto SM = [&](bool first) __attribute__((always_inline)) {
    SB(); __builtin_amdgcn_s_setprio(2); SB();
    float pm0 = max3f(S[0][0], S[0][1], S[1][0]), pm1 = max3f(S[0][2], S[0][3], S[1][1]);
    pm0 = max3f(pm0, S[1][2], S[1][3]);
#pragma unroll
    for (int e = 4; e < 16; e += 4) {
      pm0 = max3f(pm0, S[0][e], S[0][e + 1]); pm1 = max3f(pm1, S[0][e + 2], S[0][e + 3]);
      pm0 = max3f(pm0, S[1][e], S[1][e + 1]); pm1 = max3f(pm1, S[1][e + 2], S[1][e + 3]);
    }
    float pm = max2f(pm0, pm1);
    { auto rr = __builtin_amdgcn_permlane32_swap(__float_as_uint(pm), __float_as_uint(pm), false, false); pm = max2f(__uint_as_float(rr[0]), __uint_as_float(rr[1])); }
    if (first || __builtin_expect(__any(!(pm <= THR)), 0)) {
      const float dl = first ? pm : max2f(pm, 0.f);
      m += dl;
      if (!first) {
        const float al = __builtin_amdgcn_exp2f(-dl);
        l *= al;
#pragma unroll
        for (int db = 0; db < 4; ++db)
#pragma unroll
          for (int e = 0; e < 16; ++e) o[db][e] *= al;
      }
#pragma unroll
      for (int e = 0; e < 16; ++e) { S[0][e] -= dl; S[1][e] -= dl; negm[e] = -m; }
    }
    float l0 = 0.f, l1 = 0.f;
#pragma unroll
    for (int kb = 0; kb < 2; ++kb) {
#pragma unroll
      for (int e = 0; e < 16; e += 2) {
        S[kb][e] = __builtin_amdgcn_exp2f(S[kb][e]); l0 += S[kb][e];
        S[kb][e + 1] = __builtin_amdgcn_exp2f(S[kb][e + 1]); l1 += S[kb][e + 1];
      }
#pragma unroll
      for (int u = 0; u < 2; ++u) {
        u32x4 w;
#pragma unroll
        for (int e = 0; e < 4; ++e) w[e] = cvtpk(S[kb][8 * u + 2 * e], S[kb][8 * u + 2 * e + 1]);
        pf[2 * kb + u] = __builtin_bit_cast(bf16x8, w);
      }
    }
    l += l0 + l1;
    asm volatile("" : "+v"(pf[0]), "+v"(pf[1]), "+v"(pf[2]), "+v"(pf[3]), "+v"(l));
    SB(); __builtin_amdgcn_s_setprio(0); SB();
  };
  auto waitB = [&](int t) __attribute__((always_inline)) { if (t == 8) WAITV(0); else if (t == 7) WAITV(2); else if (t == 0) WAITV(6); else WAITV(4); BARRIER(); };
  if (hsel == 0) {
    unsigned sK = 9u, sV = 2u;
    auto stepA = [&](int t, auto firstc) __attribute__((always_inline)) {
      constexpr bool FIRST = decltype(firstc)::value;
      waitB(t);

      slots();
      setK(L0 + sK * SLOT);
#pragma unroll
      for (int i = 0; i < 4; ++i) {
        SB();
#pragma unroll
        for (int f = i * DL / 4; f < (i + 1) * DL / 4; ++f) rdK(f);
        SB();
        if (!FIRST) o[i] = MFMA32(vf[(12 + i) % DL], pf[3], o[i]);
      }
      SB();
      setV(L0 + sV * SLOT);
      QK(t, 0, true, FIRST);
      SM(FIRST);
#pragma unroll
      for (int f = 0; f < 12; ++f) {
        SB();
        if (FIRST && f < 4) o[f & 3] = MFMA32(vf[f % DL], pf[f >> 2], f32x16{}); else o[f & 3] = MFMA32(vf[f % DL], pf[f >> 2], o[f & 3]);
        SB();
        if (f + DL < 16) rdV(f + DL);
        SB();
        if ((f & 3) == 1) issueB(t, 4 + (f >> 2));
        if (f == 11) issueB(t, 7);
      }
      SB();
      sK += 4u; if (sK >= 10u) sK -= 10u;
      sV += 4u; if (sV >= 10u) sV -= 10u;
    };
    stepA(0, std::true_type{});
#pragma unroll 1
    for (int t = 1; t < 8; ++t) stepA(t, std::false_type{});
    waitB(8);

#pragma unroll
    for (int i = 0; i < 4; ++i) o[i] = MFMA32(vf[(12 + i) % DL], pf[3], o[i]);
  } else {
    unsigned sK = 1u, sV = 0u;
    auto stepB = [&](int t, auto firstc, auto lastc) __attribute__((always_inline)) {
      constexpr bool FIRST = decltype(firstc)::value, LAST = decltype(lastc)::value;
      waitB(t);

      slots();
      setV(L0 + sV * SLOT);
#pragma unroll
      for (int f = 0; f < DL; ++f) rdV(f);
      if (!LAST) setK(L0 + sK * SLOT);
      SB();
      if (!LAST) SM(FIRST);
#pragma unroll
      for (int f = 0; f < 16; ++f) {
        SB();
        if (FIRST && f < 4) o[f & 3] = MFMA32(vf[f % DL], pf[f >> 2], f32x16{}); else o[f & 3] = MFMA32(vf[f % DL], pf[f >> 2], o[f & 3]);
        SB();
        if (f + DL < 16) rdV(f + DL); else if (!LAST) rdK(f + DL - 16);
        SB();
        if ((f & 3) == 1 && !LAST) issueB(t, f >> 2);
      }
      if (!LAST) QK(t, 4, false, false);
      SB();
      sK += 4u; if (sK >= 10u) sK -= 10u;
      sV += 4u; if (sV >= 10u) sV -= 10u;
    };
    {
      waitB(0);

      slots();
      setK(L0 + sK * SLOT);
#pragma unroll
      for (int f = 0; f < DL; ++f) rdK(f);
#pragma unroll
      for (int j = 0; j < 4; ++j) issueB(0, j);
      QK(0, 4, false, true);
      sK = 5u; sV = 4u;
    }
    stepB(1, std::true_type{}, std::false_type{});
#pragma unroll 1
    for (int t = 2; t < 8; ++t) stepB(t, std::false_type{}, std::false_type{});
    SM(false);
    stepB(8, std::false_type{}, std::true_type{});
  }

  l = swapsum(l);
  const float inv = __builtin_amdgcn_rcpf(l);
  const rsrc_t rob = mkrsrc(Ows + (size_t)((br * NB + b) * NH + (h0 + hsel)) * 32768u, 32768u);
  const unsigned obo = (2u * qb + (l31 >> 4)) * 4096u + (l15 + 16u * hi) * 16u;
  auto ochunk = [&](unsigned c) __attribute__((always_inline)) { return L0 + (c < 3u ? 49152u + 8192u * c : 110592u + 8192u * (c - 3u)); };
  const unsigned olds = ochunk(wid) + (l31 >> 4) * 4096u + (l15 + 16u * hi) * 16u;
#pragma unroll
  for (int db = 0; db < 4; ++db)
#pragma unroll
    for (int u = 0; u < 2; ++u) {
      u32x4 w;
#pragma unroll
      for (int e = 0; e < 4; ++e) w[e] = cvtpk(o[db][8 * u + 2 * e] * inv, o[db][8 * u + 2 * e + 1] * inv);
      __builtin_amdgcn_raw_buffer_store_b128(w, rob, obo + db * 1024 + u * 512, 0, 16);
      *(LAS u32x4*)(size_t)(olds + db * 1024 + u * 512) = w;
    }

  {
  char* ws2 = Ows;
  const unsigned j = hp, kh = hsel;

  const rsrc_t rw = mkrsrc(br ? woB : woA, NH * DH * DM * 2);
  const rsrc_t ro = mkrsrc(ws2 + WS_O + (size_t)(br * NB + b) * NH * 32768u, NH * 32768u);
  unsigned vw[5];
#pragma unroll
  for (unsigned i = 0; i < 5; ++i) { const unsigned c = 64u * (qb + 4u * i) + lane; vw[i] = (c / 18u) * 1152u + (c % 18u) * 16u; }
  auto kpos = [&](int u) __attribute__((always_inline)) { return ((unsigned)u + 2u * j) & 7u; };
  const bool lowq = qb < 2u;
  const unsigned ldst = L0 + kh * 73728u + qb * 1024u;
  auto stage1 = [&](int u, int i) __attribute__((always_inline)) {
    if (i == 4 && !lowq) return;
    const unsigned so = ((hg * 8u + kpos(u)) * 128u + kh * 64u) * 1152u + j * 288u;
    dma16<W2_NT, 0>(ldst + (unsigned)(u & 3) * 18432u + (unsigned)i * 4096u, vw[i], rw, so);
  };
  const unsigned vo0 = lane * 16u + 2u * qb * 4096u, vo1 = vo0 + 4096u;
  bf16x8 bf[4][4];
  auto loadB = [&](int u, int f) __attribute__((always_inline)) {
    const unsigned so = (hg * 8u + kpos(u)) * 32768u + 2u * kh * 1024u;
    if (f == 0) asm volatile("buffer_load_dwordx4 %0, %1, %2, %3 offen sc1" : "=v"(bf[u & 3][0]) : "v"(vo0), "s"(ro), "s"(so) : "memory");
    if (f == 1) asm volatile("buffer_load_dwordx4 %0, %1, %2, %3 offen sc1" : "=v"(bf[u & 3][1]) : "v"(vo1), "s"(ro), "s"(so) : "memory");
    if (f == 2) asm volatile("buffer_load_dwordx4 %0, %1, %2, %3 offen offset:1024 sc1" : "=v"(bf[u & 3][2]) : "v"(vo0), "s"(ro), "s"(so) : "memory");
    if (f == 3) asm volatile("buffer_load_dwordx4 %0, %1, %2, %3 offen offset:1024 sc1" : "=v"(bf[u & 3][3]) : "v"(vo1), "s"(ro), "s"(so) : "memory");
  };
  auto issue = [&](int u, int v) __attribute__((always_inline)) {
    if (u > 7) return;
    if (v < 4) loadB(u, v); else if (v < 9) stage1(u, v - 4);
  };
  const unsigned aw = L0 + 288u * (16u * (lane >> 5) + 4u * ((lane >> 4) & 1u) + ((lane & 15u) >> 2)) + 8u * (lane & 3u) + kh * 73728u;
  f32x4 acc[9][2] = {};
  constexpr int DW = 6;
  bf16x8 fa[DW];
  auto rdW = [&](int F) __attribute__((always_inline)) {
    const int u = F / 18, e = (F % 18) / 9, nb = F % 9;
    const unsigned buf = aw + (unsigned)(u & 3) * 18432u + (unsigned)e * 9216u;
    fa[F % DW] = cat(lds_tr(buf + 32u * nb), lds_tr(buf + 288u * 8u + 32u * nb));
  };
  auto waitC = [&](int u, int N) __attribute__((always_inline)) {
    bf16x8* c = bf[u & 3];
    if (N == 0) WAITV(0);
    else { if (lowq) WAITV(9); else WAITV(8); }
    asm volatile("s_barrier" : "+v"(c[0]), "+v"(c[1]), "+v"(c[2]), "+v"(c[3]) :: "memory");
  };
  const unsigned long long tag = ((unsigned long long)hip_dispatch_id() + 1ull) * 0x9E3779B97F4A7C15ull ^ 0x6A09E667F3BCC908ull;
  GAS unsigned long long* fo = (GAS unsigned long long*)(ws2 + WS_FO + (size_t)((br * NB + b) * 4u + hg) * 16u * 128u);
#pragma unroll
  for (int i = 0; i < 5; ++i) stage1(0, i);
  BARRIER();
#pragma unroll
  for (int i = 0; i < 5; ++i) stage1(1, i);
#pragma unroll
  for (int u = 0; u < 2; ++u)
#pragma unroll
    for (int f = 0; f < 4; ++f) bf[u][f] = lds_b128(ochunk(4u * u + qb) + (unsigned)(f & 1) * 4096u + (2u * kh + (unsigned)(f >> 1)) * 1024u + lane * 16u);
  if (lowq) WAITV(5); else WAITV(4);
  BARRIER();
  if (wid == 0u && lane < 4u && lane != j) __hip_atomic_store(fo + (lane * 4u + j) * 16u, tag, __ATOMIC_RELAXED, __HIP_MEMORY_SCOPE_AGENT);

  auto pollO = [&]() __attribute__((always_inline)) {
    if (wid == 0u) {
      const unsigned pl = lane & 3u;
      unsigned spins = 0;
      for (;;) {
        const unsigned long long v = pl == j ? tag : __hip_atomic_load(fo + (j * 4u + pl) * 16u, __ATOMIC_RELAXED, __HIP_MEMORY_SCOPE_AGENT);
        if (__all(v == tag) || ++spins > (1u << 18)) break;
        __builtin_amdgcn_s_sleep(1);
      }
      if (lane < 4u && lane != j) __hip_atomic_store(fo + (j * 4u + lane) * 16u, 0ull, __ATOMIC_RELAXED, __HIP_MEMORY_SCOPE_AGENT);
      WAITV(0);
    }
  };
#pragma unroll
  for (int F = 0; F < DW; ++F) rdW(F);
  sfor<144>([&](auto Fc) __attribute__((always_inline)) {
    constexpr int F = decltype(Fc)::value, u = F / 18, p = F % 18, e = p / 9, nb = p % 9;
    if constexpr (p == 18 - DW && u == 0) {
      SB(); pollO();
      if (lowq) WAITV(5); else WAITV(4);
      asm volatile("s_barrier" : "+v"(bf[1][0]), "+v"(bf[1][1]), "+v"(bf[1][2]), "+v"(bf[1][3]) :: "memory");

    }
    if constexpr (p == 18 - DW && u > 0 && u < 7) { SB(); waitC(u + 1, u + 2 <= 7 ? 1 : 0); }
    SB();
    acc[nb][0] = MFMA16(fa[F % DW], bf[u & 3][2 * e], acc[nb][0]);
    acc[nb][1] = MFMA16(fa[F % DW], bf[u & 3][2 * e + 1], acc[nb][1]);
    SB();
    if constexpr (F + DW < 144) rdW(F + DW);
    SB();
    if constexpr (u == 0) { if constexpr (p >= 4 && p < 9) issue(2, p); if constexpr (p >= 18 - DW && p < 18 - DW + 4) issue(2, p - (18 - DW)); }
    else if constexpr (p < 9) issue(u + 2, p);
  });
  SB();
  BARRIER();

  LAS f32x4* cb = (LAS f32x4*)(size_t)(L0 + wid * 9216u + lane * 16u);
  LAS f32x4* cr = (LAS f32x4*)(size_t)(L0 + (wid ^ 4u) * 9216u + lane * 16u);
#pragma unroll
  for (int nb = 0; nb < 9; ++nb) cb[nb * 64] = kh ? acc[nb][0] : acc[nb][1];
  BARRIER();
  f32x4 tot[9];
#pragma unroll
  for (int nb = 0; nb < 9; ++nb) tot[nb] = kh ? cr[nb * 64] + acc[nb][1] : acc[nb][0] + cr[nb * 64];

  const unsigned grp = (br * NB + b) * 4u + j;
  const rsrc_t rp = mkrsrc(ws2 + WS_P + (size_t)grp * 16u * 18432u, 16u * 18432u);
  GAS unsigned long long* fp = (GAS unsigned long long*)(ws2 + WS_FP + (size_t)grp * 16u * 128u);
  LAS f32x4* own = (LAS f32x4*)(size_t)(L0 + 73728u + lane * 16u);
  if (qb != hg) {
#pragma unroll
    for (int nb = 0; nb < 9; ++nb)
      __builtin_amdgcn_raw_buffer_store_b128(__builtin_bit_cast(u32x4, tot[nb]), rp, lane * 16u + (unsigned)nb * 1024u, (qb * 4u + hg) * 18432u + kh * 9216u, 16);

    WAITV(0);

  } else {
#pragma unroll
    for (int nb = 0; nb < 9; ++nb) own[(kh * 9u + nb) * 64] = tot[nb];
  }
  BARRIER();
  if (wid == 0u) {
    if (lane < 4u && lane != hg) __hip_atomic_store(fp + (lane * 4u + hg) * 16u, tag, __ATOMIC_RELAXED, __HIP_MEMORY_SCOPE_AGENT);
    const unsigned pl = lane & 3u;
    unsigned spins = 0;
    for (;;) {
      const unsigned long long v = pl == hg ? tag : __hip_atomic_load(fp + (hg * 4u + pl) * 16u, __ATOMIC_RELAXED, __HIP_MEMORY_SCOPE_AGENT);
      if (__all(v == tag) || ++spins > (1u << 18)) break;
      __builtin_amdgcn_s_sleep(1);
    }
    if (lane < 4u && lane != hg) __hip_atomic_store(fp + (hg * 4u + lane) * 16u, 0ull, __ATOMIC_RELAXED, __HIP_MEMORY_SCOPE_AGENT);
  }

  BARRIER();

  {
    u32x4 pr[3][3];
    const bool third = wid < 2u;
#pragma unroll
    for (int i = 0; i < 3; ++i) {
      const unsigned tau = (i < 2 || third) ? wid + 8u * i : wid;
#pragma unroll
      for (unsigned s = 0; s < 3; ++s) { const unsigned src = s + (s >= hg ? 1u : 0u); pr[i][s] = __builtin_amdgcn_raw_buffer_load_b128(rp, lane * 16u + tau * 1024u + (hg * 4u + src) * 18432u, 0, 16); }
    }
#pragma unroll
    for (int i = 0; i < 3; ++i) {
      if (i < 2 || third) {
        const unsigned tau = wid + 8u * i;
        const f32x4 v = own[tau * 64];
        const f32x4 p0 = __builtin_bit_cast(f32x4, pr[i][0]), p1 = __builtin_bit_cast(f32x4, pr[i][1]), p2 = __builtin_bit_cast(f32x4, pr[i][2]);
        const f32x4 t0 = hg == 0 ? v : p0, t1 = hg == 0 ? p0 : (hg == 1 ? v : p1), t2 = hg <= 1 ? p1 : (hg == 2 ? v : p2), t3 = hg == 3 ? v : p2;
        const f32x4 sum = (t0 + t1) + (t2 + t3);
        const unsigned qi = tau / 9u, nb = tau % 9u;
        const unsigned q = 32u * hg + 16u * qi + (lane & 15u), n = br * 576u + j * 144u + nb * 16u + 4u * (lane >> 4);
        u32x2 w = {cvtpk(sum[0], sum[1]), cvtpk(sum[2], sum[3])};
        *(GAS u32x2*)(out + ((size_t)(b * SQ + q) * 1152u + n) * 2u) = w;
      }
    }
  }

  }
}

extern "C" void kernel_launch(void* const* d_in, const int* in_sizes, int n_in, void* d_out, int out_size, void* d_ws, size_t ws_size, hipStream_t stream) {
  (void)in_sizes; (void)n_in; (void)out_size; (void)ws_size;
  (void)hipFuncSetAttribute((const void*)k_fused, hipFuncAttributeMaxDynamicSharedMemorySize, 163840);
  k_fused<<<256, 512, 163840, stream>>>((const char*)d_in[0], (const char*)d_in[5], (const char*)d_in[1], (const char*)d_in[6], (const char*)d_in[2], (const char*)d_in[7],
                                        (const char*)d_in[3], (const char*)d_in[8], (const char*)d_in[4], (const char*)d_in[9], (char*)d_ws + WS_O, (char*)d_out);
}
```
